```python
import torch

def _softmax(x, axis=-1):
    return torch.softmax(x, dim=axis)
def _gelu(x):
    return torch.nn.functional.gelu(x, approximate="tanh")
def _silu(x):
    return torch.nn.functional.silu(x)
def _sigmoid(x):
    return torch.sigmoid(x)
def _layernorm(x, axis=-1, eps=1e-5):
    m = x.mean(dim=axis, keepdim=True)
    v = x.var(dim=axis, keepdim=True, unbiased=False)
    return (x - m) / torch.sqrt(v + eps)
def _scatter_add(tgt, idx, src, axis=0):
    return tgt.clone().index_add_(axis, idx, src)
def _segment_sum(src, seg, n):
    out = torch.zeros((n,) + src.shape[1:], dtype=src.dtype)
    return out.index_add_(0, seg, src)
def _pad(x, pw):
    flat = tuple(v for pair in reversed(pw) for v in pair)
    return torch.nn.functional.pad(x, flat)

kernel_name = 'interleave_layers__mha_block_cached__glu_gated_reglu__decode'

def setup_inputs():

    _w = (lambda a: (lambda t: t.to(torch.bfloat16) if t.is_floating_point() else t)(torch.as_tensor(a)))
    def _A_setup():
        B, Sq, D = 4, 1, 448
        H, Sk, Dh = 16, 4096, 256
        return {
            "x": _w((lambda *s: torch.randn(*s, dtype=torch.bfloat16) * 0.05)(B, Sq, D)),
            "k_cache": _w((lambda *s: torch.randn(*s, dtype=torch.bfloat16) * 0.8)(B, H, Sk, Dh)),
            "v_cache": _w((lambda *s: torch.randn(*s, dtype=torch.bfloat16) * 0.8)(B, H, Sk, Dh)),
            "wq": _w((lambda *s: torch.randn(*s, dtype=torch.bfloat16) * 0.8)(D, H * Dh) * (D ** -0.5)),
            "wo": _w((lambda *s: torch.randn(*s, dtype=torch.bfloat16) * 0.8)(H * Dh, D) * ((H * Dh) ** -0.5)),
        }
    _A_in = _A_setup()

    def _B_setup():
        B, S, D, F = 4, 1, 448, 1792
        return {
            "x": _w((lambda *s: torch.randn(*s, dtype=torch.bfloat16) * 0.05)(B, S, D)),
            "W_gate": _w((lambda *s: torch.randn(*s, dtype=torch.bfloat16) * 0.2)(D, F) * (2.0 / D) ** 0.5),
            "W_up": _w((lambda *s: torch.randn(*s, dtype=torch.bfloat16) * 0.2)(D, F) * (2.0 / D) ** 0.5),
            "W_down": _w((lambda *s: torch.randn(*s, dtype=torch.bfloat16) * 0.2)(F, D) * (1.0 / F) ** 0.5),
        }
    _B_in = _B_setup()

    return {
        "A_x": _A_in["x"],
        "A_k_cache": _A_in["k_cache"],
        "A_v_cache": _A_in["v_cache"],
        "A_wq": _A_in["wq"],
        "A_wo": _A_in["wo"],
        "B_x": _B_in["x"],
        "B_W_gate": _B_in["W_gate"],
        "B_W_up": _B_in["W_up"],
        "B_W_down": _B_in["W_down"],
    }

def reference(A_x, A_k_cache, A_v_cache, A_wq, A_wo, B_x, B_W_gate, B_W_up, B_W_down):
    def _A_ref(x, k_cache, v_cache, wq, wo):
        B, Sq, D = x.shape
        H, Dh = 16, 256
        q = (lambda x, *d: x.permute(*d))((x @ wq).reshape(B, Sq, H, Dh), 0, 2, 1, 3)
        scores = torch.einsum("bhqd,bhkd->bhqk", q, k_cache) * (Dh ** -0.5)
        p = _softmax(scores, axis=-1)
        o = torch.einsum("bhqk,bhkd->bhqd", p, v_cache)
        return (lambda x, *d: x.permute(*d))(o, 0, 2, 1, 3).reshape(B, Sq, H * Dh) @ wo

    def _B_ref(x, W_gate, W_up, W_down):
        gate = torch.einsum("bsd,df->bsf", x, W_gate)
        up = torch.einsum("bsd,df->bsf", x, W_up)
        gate = gate * (lambda x, dt: x.to(dt))(gate > 0, gate.dtype)
        h = gate * up
        return torch.einsum("bsf,fd->bsd", h, W_down)

    x = A_x
    x = x + _A_ref(_layernorm(x, axis=-1), A_k_cache, A_v_cache, A_wq, A_wo)
    x = x + _B_ref(_layernorm(x, axis=-1), B_W_gate, B_W_up, B_W_down)
    x = x + _A_ref(_layernorm(x, axis=-1), A_k_cache, A_v_cache, A_wq, A_wo)
    x = x + _B_ref(_layernorm(x, axis=-1), B_W_gate, B_W_up, B_W_down)
    return x
```

```cpp
#include <hip/hip_runtime.h>
#include <hip/hip_bf16.h>
#include <cstdint>

typedef unsigned short u16;
typedef unsigned u32x4 __attribute__((ext_vector_type(4)));
typedef float f32x4 __attribute__((ext_vector_type(4)));
typedef short bf16x8 __attribute__((ext_vector_type(8)));

constexpr int B = 4, D = 448, H = 16, SK = 4096, DH = 256, F = 1792, HD = H * DH;

__device__ __forceinline__ float bf2f(u16 b) { return __builtin_bit_cast(float, (unsigned)b << 16); }
__device__ __forceinline__ u16 f2bf(float f) {
  __bf16 h = (__bf16)f;
  return __builtin_bit_cast(u16, h);
}
__device__ __forceinline__ float rbf(float f) { return bf2f(f2bf(f)); }
__device__ __forceinline__ f32x4 mfma16(u32x4 a, u32x4 b, f32x4 c) {
  return __builtin_amdgcn_mfma_f32_16x16x32_bf16(__builtin_bit_cast(bf16x8, a), __builtin_bit_cast(bf16x8, b), c, 0, 0, 0);
}
__device__ __forceinline__ float wave_sum(float v) {
  for (int m = 32; m >= 1; m >>= 1) v += __shfl_xor(v, m);
  return v;
}
__device__ __forceinline__ float wave_max(float v) {
  for (int m = 32; m >= 1; m >>= 1) v = fmaxf(v, __shfl_xor(v, m));
  return v;
}

__device__ __forceinline__ void ln_to_lds(const u16* x, float* xn) {
  const int w = threadIdx.x >> 6, lane = threadIdx.x & 63;
  float v[7];
  float s = 0.f;
  for (int i = 0; i < 7; ++i) { v[i] = bf2f(x[w * D + lane + 64 * i]); s += v[i]; }
  s = wave_sum(s);
  const float mean = s * (1.0f / D);
  float q = 0.f;
  for (int i = 0; i < 7; ++i) { float d = v[i] - mean; q += d * d; }
  q = wave_sum(q);
  const float rs = 1.0f / sqrtf(q * (1.0f / D) + 1e-5f);
  for (int i = 0; i < 7; ++i) xn[w * D + lane + 64 * i] = rbf((v[i] - mean) * rs);
  __syncthreads();
}

__global__ void __launch_bounds__(256) k_qproj(const u16* x, const u16* wq, u16* q) {
  __shared__ float xn[B * D];
  __shared__ float red[4][4][64];
  ln_to_lds(x, xn);
  const int w = threadIdx.x >> 6, lane = threadIdx.x & 63;
  const int n = blockIdx.x * 64 + lane;
  float acc[4] = {0, 0, 0, 0};
  for (int k = w * 112; k < (w + 1) * 112; ++k) {
    const float wv = bf2f(wq[(size_t)k * HD + n]);
    for (int b = 0; b < 4; ++b) acc[b] += xn[b * D + k] * wv;
  }
  for (int b = 0; b < 4; ++b) red[w][b][lane] = acc[b];
  __syncthreads();
  const float r = red[0][w][lane] + red[1][w][lane] + red[2][w][lane] + red[3][w][lane];
  q[w * HD + n] = f2bf(r);
}

template <int REV>
__global__ void __launch_bounds__(256) k_attn(const u16* q, const u16* Kc, const u16* Vc, float* po, float* pml) {
  __shared__ float red[4][264];
  const int blk = REV ? (255 - blockIdx.x) : blockIdx.x;
  const int bh = blk >> 2, sp = blk & 3;
  const int b = bh >> 4, h = bh & 15;
  const int w = threadIdx.x >> 6, lane = threadIdx.x & 63, r = lane & 15, g = lane >> 4;
  const int key0 = sp * 1024 + w * 256;
  const u16* kb = Kc + ((size_t)bh * SK + key0) * DH;
  const u16* vb = Vc + ((size_t)bh * SK + key0) * DH;
  u32x4 qf[8];
  for (int i = 0; i < 8; ++i) qf[i] = *(const u32x4*)(q + b * HD + h * DH + 32 * i + 8 * g);
  f32x4 sc = {0, 0, 0, 0};
#pragma unroll 2
  for (int T = 0; T < 16; ++T) {
    u32x4 kf[8];
#pragma unroll
    for (int i = 0; i < 8; ++i) kf[i] = __builtin_nontemporal_load((const u32x4*)(kb + (size_t)(16 * T + r) * DH + 32 * i + 8 * g));
    f32x4 d = {0, 0, 0, 0};
#pragma unroll
    for (int i = 0; i < 8; ++i) d = mfma16(kf[i], qf[i], d);
    if (r == T) sc = d;
  }
  const float C2 = 0.0625f * 1.4426950408889634f;
  float m = fmaxf(fmaxf(sc[0], sc[1]), fmaxf(sc[2], sc[3]));
  m = wave_max(m);
  float p[4];
  float l = 0.f;
  unsigned pp[4];
  for (int t = 0; t < 4; ++t) {
    p[t] = __builtin_amdgcn_exp2f((sc[t] - m) * C2);
    const unsigned pb = f2bf(p[t]);
    l += bf2f(pb);
    pp[t] = pb | (pb << 16);
  }
  l = wave_sum(l);
  u32x4 mask;
  for (int wd = 0; wd < 4; ++wd) mask[wd] = (r < 8 && (r >> 1) == wd) ? ((r & 1) ? 0xffff0000u : 0x0000ffffu) : 0u;
  f32x4 o0 = {0, 0, 0, 0}, o1 = {0, 0, 0, 0};
#pragma unroll 2
  for (int T = 0; T < 16; ++T) {
    u32x4 vf[8];
#pragma unroll
    for (int t = 0; t < 4; ++t) {
      vf[2 * t] = __builtin_nontemporal_load((const u32x4*)(vb + (size_t)(16 * T + 4 * g + t) * DH + 8 * r));
      vf[2 * t + 1] = __builtin_nontemporal_load((const u32x4*)(vb + (size_t)(16 * T + 4 * g + t) * DH + 128 + 8 * r));
    }
#pragma unroll
    for (int t = 0; t < 4; ++t) {
      const unsigned pb = __shfl(pp[t], (lane & 48) | T);
      u32x4 a;
      for (int wd = 0; wd < 4; ++wd) a[wd] = pb & mask[wd];
      o0 = mfma16(a, vf[2 * t], o0);
      o1 = mfma16(a, vf[2 * t + 1], o1);
    }
  }
  if (g < 2) {
    for (int t = 0; t < 4; ++t) { red[w][8 * r + 4 * g + t] = o0[t]; red[w][128 + 8 * r + 4 * g + t] = o1[t]; }
  }
  if (lane == 0) { red[w][256] = m * C2; red[w][257] = l; }
  __syncthreads();
  const float M = fmaxf(fmaxf(red[0][256], red[1][256]), fmaxf(red[2][256], red[3][256]));
  float o = 0.f, L = 0.f;
  for (int ww = 0; ww < 4; ++ww) {
    const float s = __builtin_amdgcn_exp2f(red[ww][256] - M);
    o += s * red[ww][threadIdx.x];
    L += s * red[ww][257];
  }
  po[blk * 256 + threadIdx.x] = o;
  if (threadIdx.x == 0) { pml[blk * 2] = M; pml[blk * 2 + 1] = L; }
}

__global__ void __launch_bounds__(256) k_wo(const float* po, const float* pml, const u16* wo, const u16* x, u16* x1) {
  __shared__ float wgt[256];
  __shared__ float red[4][4][8];
  const int t = threadIdx.x;
  {
    const int bh = t >> 2;
    float M = -INFINITY;
    for (int s = 0; s < 4; ++s) M = fmaxf(M, pml[(bh * 4 + s) * 2]);
    float L = 0.f;
    for (int s = 0; s < 4; ++s) L += __builtin_amdgcn_exp2f(pml[(bh * 4 + s) * 2] - M) * pml[(bh * 4 + s) * 2 + 1];
    wgt[t] = __builtin_amdgcn_exp2f(pml[t * 2] - M) / L;
  }
  __syncthreads();
  float acc[4][8];
  for (int b = 0; b < 4; ++b) for (int j = 0; j < 8; ++j) acc[b][j] = 0.f;
  const int n0 = blockIdx.x * 8;
  for (int h = 0; h < 16; ++h) {
    const int k = h * 256 + t;
    const u32x4 wv = *(const u32x4*)(wo + (size_t)k * D + n0);
    float wf[8];
    for (int j = 0; j < 4; ++j) { wf[2 * j] = __builtin_bit_cast(float, wv[j] << 16); wf[2 * j + 1] = __builtin_bit_cast(float, wv[j] & 0xffff0000u); }
    for (int b = 0; b < 4; ++b) {
      float o = 0.f;
      for (int s = 0; s < 4; ++s) { const int blk = (b * 16 + h) * 4 + s; o += wgt[blk] * po[blk * 256 + t]; }
      o = rbf(o);
      for (int j = 0; j < 8; ++j) acc[b][j] += o * wf[j];
    }
  }
  const int w = t >> 6, lane = t & 63;
  for (int b = 0; b < 4; ++b) for (int j = 0; j < 8; ++j) { float v = wave_sum(acc[b][j]); if (lane == 0) red[w][b][j] = v; }
  __syncthreads();
  if (t < 32) {
    const int b = t >> 3, j = t & 7;
    const float v = red[0][b][j] + red[1][b][j] + red[2][b][j] + red[3][b][j];
    x1[b * D + n0 + j] = f2bf(bf2f(x[b * D + n0 + j]) + rbf(v));
  }
}

__global__ void __launch_bounds__(256) k_ffn1(const u16* x, const u16* wg, const u16* wu, u16* hbuf) {
  __shared__ float xn[B * D];
  __shared__ float red[4][8][64];
  ln_to_lds(x, xn);
  const int w = threadIdx.x >> 6, lane = threadIdx.x & 63;
  const int n = blockIdx.x * 64 + lane;
  float ag[4] = {0, 0, 0, 0}, au[4] = {0, 0, 0, 0};
  for (int k = w * 112; k < (w + 1) * 112; ++k) {
    const float g = bf2f(wg[(size_t)k * F + n]), u = bf2f(wu[(size_t)k * F + n]);
    for (int b = 0; b < 4; ++b) { ag[b] += xn[b * D + k] * g; au[b] += xn[b * D + k] * u; }
  }
  for (int b = 0; b < 4; ++b) { red[w][b][lane] = ag[b]; red[w][4 + b][lane] = au[b]; }
  __syncthreads();
  const float g = rbf(red[0][w][lane] + red[1][w][lane] + red[2][w][lane] + red[3][w][lane]);
  const float u = rbf(red[0][4 + w][lane] + red[1][4 + w][lane] + red[2][4 + w][lane] + red[3][4 + w][lane]);
  hbuf[w * F + n] = f2bf((g > 0.f ? g : 0.f) * u);
}

__global__ void __launch_bounds__(256) k_ffn2(const u16* hbuf, const u16* wd, const u16* x1, u16* x2) {
  __shared__ float red[4][4][8];
  const int t = threadIdx.x;
  float acc[4][8];
  for (int b = 0; b < 4; ++b) for (int j = 0; j < 8; ++j) acc[b][j] = 0.f;
  const int n0 = blockIdx.x * 8;
  for (int i = 0; i < 7; ++i) {
    const int k = i * 256 + t;
    const u32x4 wv = *(const u32x4*)(wd + (size_t)k * D + n0);
    float wf[8];
    for (int j = 0; j < 4; ++j) { wf[2 * j] = __builtin_bit_cast(float, wv[j] << 16); wf[2 * j + 1] = __builtin_bit_cast(float, wv[j] & 0xffff0000u); }
    for (int b = 0; b < 4; ++b) {
      const float hv = bf2f(hbuf[b * F + k]);
      for (int j = 0; j < 8; ++j) acc[b][j] += hv * wf[j];
    }
  }
  const int w = t >> 6, lane = t & 63;
  for (int b = 0; b < 4; ++b) for (int j = 0; j < 8; ++j) { float v = wave_sum(acc[b][j]); if (lane == 0) red[w][b][j] = v; }
  __syncthreads();
  if (t < 32) {
    const int b = t >> 3, j = t & 7;
    const float v = red[0][b][j] + red[1][b][j] + red[2][b][j] + red[3][b][j];
    x2[b * D + n0 + j] = f2bf(bf2f(x1[b * D + n0 + j]) + rbf(v));
  }
}

extern "C" void kernel_launch(void* const* d_in, const int* in_sizes, int n_in, void* d_out, int out_size, void* d_ws, size_t ws_size,
                              hipStream_t stream) {
  const u16* x = (const u16*)d_in[0];
  const u16* Kc = (const u16*)d_in[1];
  const u16* Vc = (const u16*)d_in[2];
  const u16* wq = (const u16*)d_in[3];
  const u16* wo = (const u16*)d_in[4];
  const u16* wg = (const u16*)d_in[6];
  const u16* wu = (const u16*)d_in[7];
  const u16* wd = (const u16*)d_in[8];
  char* ws = (char*)d_ws;
  u16* q = (u16*)ws;
  float* po = (float*)(ws + 65536);
  float* pml = (float*)(ws + 65536 + 262144);
  u16* x1 = (u16*)(ws + 400000 + 0);
  u16* x2 = (u16*)(ws + 400000 + 4096);
  u16* x3 = (u16*)(ws + 400000 + 8192);
  u16* hb = (u16*)(ws + 400000 + 16384);
  u16* out = (u16*)d_out;
  k_qproj<<<64, 256, 0, stream>>>(x, wq, q);
  k_attn<0><<<256, 256, 0, stream>>>(q, Kc, Vc, po, pml);
  k_wo<<<56, 256, 0, stream>>>(po, pml, wo, x, x1);
  k_ffn1<<<28, 256, 0, stream>>>(x1, wg, wu, hb);
  k_ffn2<<<56, 256, 0, stream>>>(hb, wd, x1, x2);
  k_qproj<<<64, 256, 0, stream>>>(x2, wq, q);
  k_attn<0><<<256, 256, 0, stream>>>(q, Kc, Vc, po, pml);
  k_wo<<<56, 256, 0, stream>>>(po, pml, wo, x2, x3);
  k_ffn1<<<28, 256, 0, stream>>>(x3, wg, wu, hb);
  k_ffn2<<<56, 256, 0, stream>>>(hb, wd, x3, out);
}
```

```cpp
#include <hip/hip_runtime.h>
#include <hip/hip_bf16.h>
#include <cstdint>

typedef unsigned short u16;
constexpr int PREC = 640, Q_OFF = 65536, P_OFF = 327680, RDY_OFF = 4096, DBG_OFF = 4194304, WS_BYTES = 8388608;

__global__ void __launch_bounds__(256, 1)
fused_layers(const u16* x, const u16* Kc, const u16* Vc, const u16* wq, const u16* wo, const u16* wg, const u16* wu, const u16* wd,
             u16* out, unsigned char* ws) {
  __shared__ __attribute__((aligned(16))) unsigned char smem[163840];
  const int w = __builtin_amdgcn_readfirstlane(threadIdx.x >> 6);
  const int xg = blockIdx.x & 7, bi = blockIdx.x >> 3;
  const unsigned lds0 = (unsigned)(uintptr_t)(__attribute__((address_space(3))) unsigned char*)smem;
  if (bi >= 4) {
    const int si = bi - 4, hh = si >= 14 ? 1 : 0, jj = si - 14 * hh, h = 2 * ((xg + 0) & 7) + hh;
    const int wi = jj * 4 + w;
    const int u0 = (wi * 128) / 7, u1 = (wi * 128 + 128) / 7, uc = (jj * 512) / 7;
    const int has19 = u1 - u0 - 18;
    const int base = (h * 1024 + wi * 1) * 2048, opto = (1008 + (u0 - 18 * wi) - wi * 1) * 2048;
    const int lopt = has19 ? 139264 + (u0 - uc - 18 * w) * 2048 : w * 34816;
    const int rdy = RDY_OFF + (32 + h * 14 + jj) * 128;
    const int qoff = Q_OFF + h * 2048, poff = P_OFF + (h * 14 + jj) * PREC;
    const int dbg = DBG_OFF + (blockIdx.x * 4 + w) * 512;
    asm volatile(
        "s_load_dword s78, %2, 0\n"
        "s_memrealtime s[84:85]\n"
        "s_mov_b32 s79, %12\n"
        "s_mov_b64 s[36:37], %0\n"
        "s_mov_b64 s[40:41], %1\n"
        "s_mov_b64 s[44:45], %2\n"
        "s_and_b32 s37, s37, 0xffff\n"
        "s_mov_b32 s39, 0x20000\n"
        "s_and_b32 s41, s41, 0xffff\n"
        "s_mov_b32 s43, 0x20000\n"
        "s_and_b32 s45, s45, 0xffff\n"
        "s_mov_b32 s47, 0x20000\n"
        "s_mov_b32 s38, 0x8000000\n"
        "s_mov_b32 s42, 0x8000000\n"
        "s_mov_b32 s46, %9\n"
        "s_mov_b32 s90, %9\n"
        "s_mov_b32 s48, %3\n"
        "s_add_u32 s49, s48, 0x2000000\n"
        "s_mov_b32 s52, %4\n"
        "s_mul_i32 s53, %5, 34816\n"
        "s_lshl_b32 s74, %5, 3\n"
        "s_add_u32 s76, s53, 16384\n"
        "s_add_u32 s77, s53, 22528\n"
        "s_add_u32 s75, s53, 28672\n"
        "s_mov_b32 s57, 0x3db8aa3b\n"
        "s_mov_b32 s54, %6\n"
        "s_mov_b32 s55, %7\n"
        "s_mov_b32 s56, %8\n"
        "s_mov_b32 s65, %10\n"
        "s_mov_b32 s51, 0\n"
        "s_mov_b32 s61, 0\n"
        "s_mov_b64 exec, -1\n"
        "s_mov_b32 s51, 4\n"
        "s_mov_b32 s51, 0\n"
        "v_mbcnt_lo_u32_b32 v16, -1, 0\n"
        "v_mbcnt_hi_u32_b32 v16, -1, v16\n"
        "v_and_b32 v17, 15, v16\n"
        "v_lshrrev_b32 v18, 4, v16\n"
        "v_and_b32 v19, 3, v16\n"
        "v_lshl_add_u32 v19, v18, 2, v19\n"
        "v_lshlrev_b32 v0, 4, v19\n"
        "v_lshrrev_b32 v19, 2, v17\n"
        "v_lshl_add_u32 v0, v19, 9, v0\n"
        "v_lshlrev_b32 v2, 4, v16\n"
        "s_mov_b32 s83, 0x20000\n"
        "s_lshl_b32 s58, %18, 2\n"
        "s_min_u32 s58, s58, 24\n"
        "s_getpc_b64 s[66:67]\n"
        ".Lst_pc_%=:\n"
        "s_and_b32 s66, s66, 0xffffff80\n"
        "s_lshl_b32 s50, %19, 10\n"
        "s_max_i32 s70, 0, .Lfused_code_end-.Lst_pc_%=\n"
        "v_add_u32 v6, s50, v2\n"
        "s_mov_b32 s50, 0\n"
        "s_mov_b64 s[80:81], s[66:67]\n"
        "s_and_b32 s81, s81, 0xffff\n"
        "s_mov_b32 s82, s70\n"
        "s_add_u32 s59, s50, 0\n"
        "buffer_load_dwordx4 v[192:195], v6, s[80:83], s59 offen\n"
        "v_lshlrev_b32 v5, 25, v16\n"
        "s_mov_b64 exec, 15\n"
        "buffer_load_dword v190, v5, s[36:39], s48 offen nt\n"
        "buffer_load_dword v191, v5, s[40:43], s48 offen nt\n"
        "s_mov_b64 exec, -1\n"
        "s_lshl_b32 s50, %19, 15\n"
        "s_lshl_b32 s59, %18, 10\n"
        "s_add_u32 s50, s50, s59\n"
        "s_mov_b64 s[80:81], %13\n"
        "s_and_b32 s81, s81, 0xffff\n"
        "s_mov_b32 s82, 3670016\n"
        "s_add_u32 s59, s50, 0\n"
        "buffer_load_dwordx4 v[196:199], v2, s[80:83], s59 offen\n"
        "s_add_u32 s59, s50, 8192\n"
        "buffer_load_dwordx4 v[200:203], v2, s[80:83], s59 offen\n"
        "s_add_u32 s59, s50, 16384\n"
        "buffer_load_dwordx4 v[204:207], v2, s[80:83], s59 offen\n"
        "s_add_u32 s59, s50, 24576\n"
        "buffer_load_dwordx4 v[208:211], v2, s[80:83], s59 offen\n"
        "s_mul_i32 s50, %18, 458752\n"
        "s_lshl_b32 s59, %19, 12\n"
        "s_add_u32 s50, s50, s59\n"
        "s_mov_b64 s[80:81], %14\n"
        "s_and_b32 s81, s81, 0xffff\n"
        "s_mov_b32 s82, 3670016\n"
        "v_lshlrev_b32 v19, 4, v17\n"
        "v_lshl_add_u32 v1, v18, 9, v19\n"
        "v_lshlrev_b32 v2, 4, v16\n"
        "v_add_u32 v3, s53, v2\n"
        "v_mov_b32 v32, -1\n"
        "v_mov_b32 v33, -1\n"
        "v_mov_b32 v34, -1\n"
        "v_mov_b32 v35, -1\n"
        "s_mul_i32 s59, s74, 35840\n"
        "s_add_u32 s59, s59, s56\n"
        "s_add_u32 s60, s59, 143360\n"
        "s_mov_b32 exec_hi, 0xff\n"
        "buffer_store_dwordx4 v[32:35], v2, s[44:47], s59 offen sc1\n"
        "buffer_store_dwordx4 v[32:35], v2, s[44:47], s60 offen sc1\n"
        "s_mov_b32 exec_hi, -1\n"
        "v_lshlrev_b32 v19, 5, v17\n"
        "v_lshl_add_u32 v19, v18, 4, v19\n"
        "s_lshl_b32 s59, s74, 7\n"
        "s_add_u32 s59, s59, 155648\n"
        "v_add_u32 v4, s59, v19\n"
        "v_and_b32 v19, 1, v17\n"
        "v_lshlrev_b32 v19, 4, v19\n"
        "v_mov_b32 v5, 0xffff\n"
        "v_lshlrev_b32 v19, v19, v5\n"
        "v_lshrrev_b32 v17, 1, v17\n"
        "v_cmp_eq_u32 vcc, 0, v17\n"
        "s_nop 1\n"
        "v_cndmask_b32 v40, 0, v19, vcc\n"
        "v_cmp_eq_u32 vcc, 1, v17\n"
        "s_nop 1\n"
        "v_cndmask_b32 v41, 0, v19, vcc\n"
        "v_cmp_eq_u32 vcc, 2, v17\n"
        "s_nop 1\n"
        "v_cndmask_b32 v42, 0, v19, vcc\n"
        "v_cmp_eq_u32 vcc, 3, v17\n"
        "s_nop 1\n"
        "v_cndmask_b32 v43, 0, v19, vcc\n"
        "v_mov_b32 v8, 0\n"
        "v_mov_b32 v9, 0\n"
        "v_mov_b32 v10, 0\n"
        "v_mov_b32 v11, 0\n"
        "v_mov_b32 v12, 0\n"
        "v_mov_b32 v13, 0\n"
        "v_mov_b32 v14, 0\n"
        "v_mov_b32 v15, 0\n"
        "s_mov_b32 s86, 0x000f000f\n"
        "s_mov_b32 s87, 0x000f000f\n"
        "v_mov_b32 v19, 159744\n"
        "v_mov_b32 v5, 0\n"
        "ds_write_b32 v19, v5 offset:32\n"
        "s_waitcnt lgkmcnt(0)\n"
        "s_add_u32 s78, s78, 1\n"
        "s_max_u32 s78, s78, 1\n"
        "s_barrier\n"
        "s_memrealtime s[84:85]\n"
        "s_waitcnt lgkmcnt(0)\n"
        "s_lshl_b32 s84, s84, 4\n"
        "s_add_u32 s84, s84, 640\n"
        "s_mov_b32 s85, 40600\n"
        "s_mov_b32 s88, s84\n"
        "v_lshrrev_b32 v5, 9, v2\n"
        "v_mul_u32_u24 v5, 3072, v5\n"
        "v_add_u32 v5, v5, v2\n"
        "s_call_b64 s[68:69], .Lst_pace_%=\n"
        "s_add_u32 s59, s50, 0\n"
        "buffer_load_dwordx4 v[212:215], v2, s[80:83], s59 offen\n"
        "s_add_u32 s59, s50, 1024\n"
        "buffer_load_dwordx4 v[216:219], v2, s[80:83], s59 offen\n"
        "s_call_b64 s[68:69], .Lst_pace_%=\n"
        "s_add_u32 s59, s50, 2048\n"
        "buffer_load_dwordx4 v[220:223], v2, s[80:83], s59 offen\n"
        "s_add_u32 s59, s50, 3072\n"
        "buffer_load_dwordx4 v[224:227], v2, s[80:83], s59 offen\n"
        "s_mul_i32 s50, %19, 14336\n"
        "s_lshl_b32 s59, s58, 7\n"
        "s_add_u32 s50, s50, s59\n"
        "s_mov_b64 s[80:81], %15\n"
        "s_and_b32 s81, s81, 0xffff\n"
        "s_mov_b32 s82, 1605632\n"
        "s_call_b64 s[68:69], .Lst_pace_%=\n"
        "s_add_u32 s59, s50, 0\n"
        "buffer_load_dwordx4 v[228:231], v5, s[80:83], s59 offen\n"
        "s_add_u32 s59, s50, 7168\n"
        "buffer_load_dwordx4 v[232:235], v5, s[80:83], s59 offen\n"
        "s_mov_b64 s[80:81], %16\n"
        "s_and_b32 s81, s81, 0xffff\n"
        "s_mov_b32 s82, 1605632\n"
        "s_call_b64 s[68:69], .Lst_pace_%=\n"
        "s_add_u32 s59, s50, 0\n"
        "buffer_load_dwordx4 v[236:239], v5, s[80:83], s59 offen\n"
        "s_add_u32 s59, s50, 7168\n"
        "buffer_load_dwordx4 v[240:243], v5, s[80:83], s59 offen\n"
        "s_mul_i32 s50, s58, 57344\n"
        "s_lshl_b32 s59, %19, 11\n"
        "s_add_u32 s50, s50, s59\n"
        "s_mov_b64 s[80:81], %17\n"
        "s_and_b32 s81, s81, 0xffff\n"
        "s_mov_b32 s82, 1605632\n"
        "s_call_b64 s[68:69], .Lst_pace_%=\n"
        "s_add_u32 s59, s50, 0\n"
        "buffer_load_dwordx4 v[244:247], v2, s[80:83], s59 offen\n"
        "s_add_u32 s59, s50, 1024\n"
        "buffer_load_dwordx4 v[248:251], v2, s[80:83], s59 offen\n"
        "s_mov_b32 s89, %21\n"
        "s_mov_b32 s60, %20\n"
        "s_mul_i32 s59, s60, 4097\n"
        "s_lshr_b32 s59, s59, 12\n"
        "s_mul_i32 s59, s59, 55\n"
        "s_add_u32 s59, s59, s60\n"
        "s_lshl_b32 s4, s59, 11\n"
        "s_add_u32 s60, s60, 1\n"
        "s_cmp_eq_u32 s60, 18\n"
        "s_cselect_b32 s60, 0, s60\n"
        "s_mul_i32 s59, s60, 4097\n"
        "s_lshr_b32 s59, s59, 12\n"
        "s_mul_i32 s59, s59, 55\n"
        "s_add_u32 s59, s59, s60\n"
        "s_lshl_b32 s5, s59, 11\n"
        "s_add_u32 s60, s60, 1\n"
        "s_cmp_eq_u32 s60, 18\n"
        "s_cselect_b32 s60, 0, s60\n"
        "s_mul_i32 s59, s60, 4097\n"
        "s_lshr_b32 s59, s59, 12\n"
        "s_mul_i32 s59, s59, 55\n"
        "s_add_u32 s59, s59, s60\n"
        "s_lshl_b32 s6, s59, 11\n"
        "s_add_u32 s60, s60, 1\n"
        "s_cmp_eq_u32 s60, 18\n"
        "s_cselect_b32 s60, 0, s60\n"
        "s_mul_i32 s59, s60, 4097\n"
        "s_lshr_b32 s59, s59, 12\n"
        "s_mul_i32 s59, s59, 55\n"
        "s_add_u32 s59, s59, s60\n"
        "s_lshl_b32 s7, s59, 11\n"
        "s_add_u32 s60, s60, 1\n"
        "s_cmp_eq_u32 s60, 18\n"
        "s_cselect_b32 s60, 0, s60\n"
        "s_mul_i32 s59, s60, 4097\n"
        "s_lshr_b32 s59, s59, 12\n"
        "s_mul_i32 s59, s59, 55\n"
        "s_add_u32 s59, s59, s60\n"
        "s_lshl_b32 s8, s59, 11\n"
        "s_add_u32 s60, s60, 1\n"
        "s_cmp_eq_u32 s60, 18\n"
        "s_cselect_b32 s60, 0, s60\n"
        "s_mul_i32 s59, s60, 4097\n"
        "s_lshr_b32 s59, s59, 12\n"
        "s_mul_i32 s59, s59, 55\n"
        "s_add_u32 s59, s59, s60\n"
        "s_lshl_b32 s9, s59, 11\n"
        "s_add_u32 s60, s60, 1\n"
        "s_cmp_eq_u32 s60, 18\n"
        "s_cselect_b32 s60, 0, s60\n"
        "s_mul_i32 s59, s60, 4097\n"
        "s_lshr_b32 s59, s59, 12\n"
        "s_mul_i32 s59, s59, 55\n"
        "s_add_u32 s59, s59, s60\n"
        "s_lshl_b32 s10, s59, 11\n"
        "s_add_u32 s60, s60, 1\n"
        "s_cmp_eq_u32 s60, 18\n"
        "s_cselect_b32 s60, 0, s60\n"
        "s_mul_i32 s59, s60, 4097\n"
        "s_lshr_b32 s59, s59, 12\n"
        "s_mul_i32 s59, s59, 55\n"
        "s_add_u32 s59, s59, s60\n"
        "s_lshl_b32 s11, s59, 11\n"
        "s_add_u32 s60, s60, 1\n"
        "s_cmp_eq_u32 s60, 18\n"
        "s_cselect_b32 s60, 0, s60\n"
        "s_mul_i32 s59, s60, 4097\n"
        "s_lshr_b32 s59, s59, 12\n"
        "s_mul_i32 s59, s59, 55\n"
        "s_add_u32 s59, s59, s60\n"
        "s_lshl_b32 s12, s59, 11\n"
        "s_add_u32 s60, s60, 1\n"
        "s_cmp_eq_u32 s60, 18\n"
        "s_cselect_b32 s60, 0, s60\n"
        "s_mul_i32 s59, s60, 4097\n"
        "s_lshr_b32 s59, s59, 12\n"
        "s_mul_i32 s59, s59, 55\n"
        "s_add_u32 s59, s59, s60\n"
        "s_lshl_b32 s13, s59, 11\n"
        "s_add_u32 s60, s60, 1\n"
        "s_cmp_eq_u32 s60, 18\n"
        "s_cselect_b32 s60, 0, s60\n"
        "s_mul_i32 s59, s60, 4097\n"
        "s_lshr_b32 s59, s59, 12\n"
        "s_mul_i32 s59, s59, 55\n"
        "s_add_u32 s59, s59, s60\n"
        "s_lshl_b32 s14, s59, 11\n"
        "s_add_u32 s60, s60, 1\n"
        "s_cmp_eq_u32 s60, 18\n"
        "s_cselect_b32 s60, 0, s60\n"
        "s_mul_i32 s59, s60, 4097\n"
        "s_lshr_b32 s59, s59, 12\n"
        "s_mul_i32 s59, s59, 55\n"
        "s_add_u32 s59, s59, s60\n"
        "s_lshl_b32 s15, s59, 11\n"
        "s_add_u32 s60, s60, 1\n"
        "s_cmp_eq_u32 s60, 18\n"
        "s_cselect_b32 s60, 0, s60\n"
        "s_mul_i32 s59, s60, 4097\n"
        "s_lshr_b32 s59, s59, 12\n"
        "s_mul_i32 s59, s59, 55\n"
        "s_add_u32 s59, s59, s60\n"
        "s_lshl_b32 s16, s59, 11\n"
        "s_add_u32 s60, s60, 1\n"
        "s_cmp_eq_u32 s60, 18\n"
        "s_cselect_b32 s60, 0, s60\n"
        "s_mul_i32 s59, s60, 4097\n"
        "s_lshr_b32 s59, s59, 12\n"
        "s_mul_i32 s59, s59, 55\n"
        "s_add_u32 s59, s59, s60\n"
        "s_lshl_b32 s17, s59, 11\n"
        "s_add_u32 s60, s60, 1\n"
        "s_cmp_eq_u32 s60, 18\n"
        "s_cselect_b32 s60, 0, s60\n"
        "s_mul_i32 s59, s60, 4097\n"
        "s_lshr_b32 s59, s59, 12\n"
        "s_mul_i32 s59, s59, 55\n"
        "s_add_u32 s59, s59, s60\n"
        "s_lshl_b32 s18, s59, 11\n"
        "s_add_u32 s60, s60, 1\n"
        "s_cmp_eq_u32 s60, 18\n"
        "s_cselect_b32 s60, 0, s60\n"
        "s_mul_i32 s59, s60, 4097\n"
        "s_lshr_b32 s59, s59, 12\n"
        "s_mul_i32 s59, s59, 55\n"
        "s_add_u32 s59, s59, s60\n"
        "s_lshl_b32 s19, s59, 11\n"
        "s_add_u32 s60, s60, 1\n"
        "s_cmp_eq_u32 s60, 18\n"
        "s_cselect_b32 s60, 0, s60\n"
        "s_mul_i32 s59, s60, 4097\n"
        "s_lshr_b32 s59, s59, 12\n"
        "s_mul_i32 s59, s59, 55\n"
        "s_add_u32 s59, s59, s60\n"
        "s_lshl_b32 s20, s59, 11\n"
        "s_add_u32 s60, s60, 1\n"
        "s_cmp_eq_u32 s60, 18\n"
        "s_cselect_b32 s60, 0, s60\n"
        "s_mul_i32 s59, s60, 4097\n"
        "s_lshr_b32 s59, s59, 12\n"
        "s_mul_i32 s59, s59, 55\n"
        "s_add_u32 s59, s59, s60\n"
        "s_lshl_b32 s21, s59, 11\n"
        "s_call_b64 s[68:69], .Lst_pace_%=\n"
        "s_add_u32 s50, s48, s4\n"
        "s_add_u32 m0, s76, 0\n"
        "s_nop 0\n"
        "buffer_load_dwordx4 v0, s[36:39], s50 offen nt lds\n"
        "s_add_u32 m0, s76, 768\n"
        "s_nop 0\n"
        "buffer_load_dwordx4 v0, s[36:39], s50 offen offset:256 nt lds\n"
        "s_call_b64 s[68:69], .Lst_pace_%=\n"
        "s_add_u32 s50, s48, s5\n"
        "s_add_u32 m0, s76, 2048\n"
        "s_nop 0\n"
        "buffer_load_dwordx4 v0, s[36:39], s50 offen nt lds\n"
        "s_add_u32 m0, s76, 2816\n"
        "s_nop 0\n"
        "buffer_load_dwordx4 v0, s[36:39], s50 offen offset:256 nt lds\n"
        "s_call_b64 s[68:69], .Lst_pace_%=\n"
        "s_add_u32 s50, s48, s6\n"
        "s_add_u32 m0, s76, 4096\n"
        "s_nop 0\n"
        "buffer_load_dwordx4 v0, s[36:39], s50 offen nt lds\n"
        "s_add_u32 m0, s76, 4864\n"
        "s_nop 0\n"
        "buffer_load_dwordx4 v0, s[36:39], s50 offen offset:256 nt lds\n"
        "s_call_b64 s[68:69], .Lst_pace_%=\n"
        "s_add_u32 s50, s48, s7\n"
        "buffer_load_dwordx4 a[0:3], v0, s[36:39], s50 offen nt\n"
        "buffer_load_dwordx4 a[4:7], v0, s[36:39], s50 offen offset:256 nt\n"
        "s_call_b64 s[68:69], .Lst_pace_%=\n"
        "s_add_u32 s50, s48, s8\n"
        "buffer_load_dwordx4 a[8:11], v0, s[36:39], s50 offen nt\n"
        "buffer_load_dwordx4 a[12:15], v0, s[36:39], s50 offen offset:256 nt\n"
        "s_call_b64 s[68:69], .Lst_pace_%=\n"
        "s_add_u32 s50, s48, s9\n"
        "buffer_load_dwordx4 a[16:19], v0, s[36:39], s50 offen nt\n"
        "buffer_load_dwordx4 a[20:23], v0, s[36:39], s50 offen offset:256 nt\n"
        "s_call_b64 s[68:69], .Lst_pace_%=\n"
        "s_add_u32 s50, s48, s10\n"
        "buffer_load_dwordx4 a[24:27], v0, s[36:39], s50 offen nt\n"
        "buffer_load_dwordx4 a[28:31], v0, s[36:39], s50 offen offset:256 nt\n"
        "s_call_b64 s[68:69], .Lst_pace_%=\n"
        "s_add_u32 s50, s48, s11\n"
        "buffer_load_dwordx4 a[32:35], v0, s[36:39], s50 offen nt\n"
        "buffer_load_dwordx4 a[36:39], v0, s[36:39], s50 offen offset:256 nt\n"
        "s_call_b64 s[68:69], .Lst_pace_%=\n"
        "s_add_u32 s50, s48, s12\n"
        "buffer_load_dwordx4 a[40:43], v0, s[36:39], s50 offen nt\n"
        "buffer_load_dwordx4 a[44:47], v0, s[36:39], s50 offen offset:256 nt\n"
        ".Lst_loop_%=:\n"
        "s_mul_i32 s62, s51, 32768\n"
        "s_add_u32 s62, s62, s55\n"
        "s_waitcnt vmcnt(16)\n"
        "s_call_b64 s[68:69], .Lst_pace_%=\n"
        "s_add_u32 s50, s48, s13\n"
        "buffer_load_dwordx4 a[48:51], v0, s[36:39], s50 offen nt\n"
        "buffer_load_dwordx4 a[52:55], v0, s[36:39], s50 offen offset:256 nt\n"
        "s_waitcnt vmcnt(16)\n"
        "s_call_b64 s[68:69], .Lst_pace_%=\n"
        "s_add_u32 s50, s48, s14\n"
        "buffer_load_dwordx4 a[56:59], v0, s[36:39], s50 offen nt\n"
        "buffer_load_dwordx4 a[60:63], v0, s[36:39], s50 offen offset:256 nt\n"
        "s_waitcnt vmcnt(16)\n"
        "s_call_b64 s[68:69], .Lst_pace_%=\n"
        "s_add_u32 s50, s48, s15\n"
        "buffer_load_dwordx4 a[64:67], v0, s[36:39], s50 offen nt\n"
        "buffer_load_dwordx4 a[68:71], v0, s[36:39], s50 offen offset:256 nt\n"
        "s_waitcnt vmcnt(16)\n"
        "s_call_b64 s[68:69], .Lst_pace_%=\n"
        "s_add_u32 s50, s48, s16\n"
        "buffer_load_dwordx4 a[72:75], v0, s[36:39], s50 offen nt\n"
        "buffer_load_dwordx4 a[76:79], v0, s[36:39], s50 offen offset:256 nt\n"
        "s_waitcnt vmcnt(16)\n"
        "s_call_b64 s[68:69], .Lst_pace_%=\n"
        "s_cmp_eq_u32 s52, 0\n"
        "s_cbranch_scc1 .Lst_noopt0_%=\n"
        "s_add_u32 s50, s48, s89\n"
        "s_add_u32 m0, s54, 4096\n"
        "s_nop 0\n"
        "buffer_load_dwordx4 v0, s[36:39], s50 offen nt lds\n"
        "s_add_u32 m0, s54, 4864\n"
        "s_nop 0\n"
        "buffer_load_dwordx4 v0, s[36:39], s50 offen offset:256 nt lds\n"
        ".Lst_noopt0_%=:\n"
        "s_add_u32 s50, s48, s17\n"
        "buffer_load_dwordx4 a[80:83], v0, s[36:39], s50 offen nt\n"
        "buffer_load_dwordx4 a[84:87], v0, s[36:39], s50 offen offset:256 nt\n"
        "s_waitcnt vmcnt(16)\n"
        "s_call_b64 s[68:69], .Lst_pace_%=\n"
        "s_add_u32 s50, s48, s18\n"
        "buffer_load_dwordx4 a[88:91], v0, s[36:39], s50 offen nt\n"
        "buffer_load_dwordx4 a[92:95], v0, s[36:39], s50 offen offset:256 nt\n"
        "s_waitcnt vmcnt(16)\n"
        "s_call_b64 s[68:69], .Lst_pace_%=\n"
        "s_add_u32 s50, s48, s19\n"
        "buffer_load_dwordx4 a[96:99], v0, s[36:39], s50 offen nt\n"
        "buffer_load_dwordx4 a[100:103], v0, s[36:39], s50 offen offset:256 nt\n"
        "s_waitcnt vmcnt(16)\n"
        "s_call_b64 s[68:69], .Lst_pace_%=\n"
        "s_add_u32 s50, s48, s20\n"
        "buffer_load_dwordx4 a[104:107], v0, s[36:39], s50 offen nt\n"
        "buffer_load_dwordx4 a[108:111], v0, s[36:39], s50 offen offset:256 nt\n"
        "s_waitcnt vmcnt(16)\n"
        "s_call_b64 s[68:69], .Lst_pace_%=\n"
        "s_add_u32 s50, s48, s21\n"
        "buffer_load_dwordx4 a[112:115], v0, s[36:39], s50 offen nt\n"
        "buffer_load_dwordx4 a[116:119], v0, s[36:39], s50 offen offset:256 nt\n"
        "s_waitcnt vmcnt(16)\n"
        "s_mov_b64 exec, s[86:87]\n"
        "buffer_load_dwordx4 v[8:11], v2, s[44:47], s62 offen offset:0 sc1\n"
        "buffer_load_dwordx4 v[12:15], v2, s[44:47], s62 offen offset:1024 sc1\n"
        "buffer_load_dwordx4 v[32:35], v2, s[44:47], s62 offen offset:128 sc1\n"
        "buffer_load_dwordx4 v[36:39], v2, s[44:47], s62 offen offset:1152 sc1\n"
        "s_mov_b64 exec, -1\n"
        "s_call_b64 s[68:69], .Lst_pace_%=\n"
        "s_cmp_eq_u32 s52, 0\n"
        "s_cbranch_scc1 .Lst_noopt1_%=\n"
        "s_add_u32 s50, s48, s89\n"
        "s_add_u32 m0, s54, 0\n"
        "s_nop 0\n"
        "buffer_load_dwordx4 v1, s[40:43], s50 offen nt lds\n"
        "s_add_u32 m0, s54, 768\n"
        "s_nop 0\n"
        "buffer_load_dwordx4 v1, s[40:43], s50 offen offset:256 nt lds\n"
        ".Lst_noopt1_%=:\n"
        "s_add_u32 s50, s48, s4\n"
        "s_add_u32 m0, s53, 0\n"
        "s_nop 0\n"
        "buffer_load_dwordx4 v1, s[40:43], s50 offen nt lds\n"
        "s_add_u32 m0, s53, 768\n"
        "s_nop 0\n"
        "buffer_load_dwordx4 v1, s[40:43], s50 offen offset:256 nt lds\n"
        "s_waitcnt vmcnt(20)\n"
        "s_call_b64 s[68:69], .Lst_pace_%=\n"
        "s_add_u32 s50, s48, s5\n"
        "s_add_u32 m0, s53, 2048\n"
        "s_nop 0\n"
        "buffer_load_dwordx4 v1, s[40:43], s50 offen nt lds\n"
        "s_add_u32 m0, s53, 2816\n"
        "s_nop 0\n"
        "buffer_load_dwordx4 v1, s[40:43], s50 offen offset:256 nt lds\n"
        "s_waitcnt vmcnt(20)\n"
        "s_call_b64 s[68:69], .Lst_pace_%=\n"
        "s_add_u32 s50, s48, s6\n"
        "s_add_u32 m0, s53, 4096\n"
        "s_nop 0\n"
        "buffer_load_dwordx4 v1, s[40:43], s50 offen nt lds\n"
        "s_add_u32 m0, s53, 4864\n"
        "s_nop 0\n"
        "buffer_load_dwordx4 v1, s[40:43], s50 offen offset:256 nt lds\n"
        "s_waitcnt vmcnt(20)\n"
        "s_call_b64 s[68:69], .Lst_pace_%=\n"
        "s_add_u32 s50, s48, s7\n"
        "buffer_load_dwordx4 a[120:123], v1, s[40:43], s50 offen nt\n"
        "buffer_load_dwordx4 a[124:127], v1, s[40:43], s50 offen offset:256 nt\n"
        "s_barrier\n"
        "s_cmp_lg_u32 s74, 0\n"
        "s_cbranch_scc1 .Lst_nordy_%=\n"
        "v_mov_b32 v16, s78\n"
        "v_not_b32 v17, v16\n"
        "s_mov_b64 exec, 1\n"
        "buffer_store_dwordx2 v[16:17], off, s[44:47], s79 sc1\n"
        "s_mov_b64 exec, -1\n"
        ".Lst_nordy_%=:\n"
        "s_waitcnt vmcnt(12)\n"
        "s_call_b64 s[68:69], .Lst_pace_%=\n"
        "s_add_u32 s50, s48, s8\n"
        "buffer_load_dwordx4 a[128:131], v1, s[40:43], s50 offen nt\n"
        "buffer_load_dwordx4 a[132:135], v1, s[40:43], s50 offen offset:256 nt\n"
        "s_waitcnt vmcnt(10)\n"
        "s_call_b64 s[66:67], .Lst_chkq1_%=\n"
        "s_waitcnt vmcnt(8)\n"
        "s_call_b64 s[68:69], .Lst_pace_%=\n"
        "s_add_u32 s50, s48, s9\n"
        "buffer_load_dwordx4 a[136:139], v1, s[40:43], s50 offen nt\n"
        "buffer_load_dwordx4 a[140:143], v1, s[40:43], s50 offen offset:256 nt\n"
        "s_cmp_lg_u32 s74, 24\n"
        "s_cbranch_scc1 .Lst_noqk0_18_%=\n"
        "s_mov_b32 s63, s76\n"
        "s_mov_b64 vcc, 0\n"
        "s_call_b64 s[66:67], .Lst_qk0_%=\n"
        ".Lst_noqk0_18_%=:\n"
        "s_waitcnt vmcnt(8)\n"
        "s_call_b64 s[68:69], .Lst_pace_%=\n"
        "s_add_u32 s50, s48, s10\n"
        "buffer_load_dwordx4 a[144:147], v1, s[40:43], s50 offen nt\n"
        "buffer_load_dwordx4 a[148:151], v1, s[40:43], s50 offen offset:256 nt\n"
        "s_waitcnt vmcnt(8)\n"
        "s_call_b64 s[68:69], .Lst_pace_%=\n"
        "s_add_u32 s50, s48, s11\n"
        "buffer_load_dwordx4 a[152:155], v1, s[40:43], s50 offen nt\n"
        "buffer_load_dwordx4 a[156:159], v1, s[40:43], s50 offen offset:256 nt\n"
        "s_cmp_lg_u32 s74, 16\n"
        "s_cbranch_scc1 .Lst_noqk0_20_%=\n"
        "s_mov_b32 s63, s76\n"
        "s_mov_b64 vcc, 0\n"
        "s_call_b64 s[66:67], .Lst_qk0_%=\n"
        ".Lst_noqk0_20_%=:\n"
        "s_waitcnt vmcnt(8)\n"
        "s_call_b64 s[68:69], .Lst_pace_%=\n"
        "s_add_u32 s50, s48, s12\n"
        "buffer_load_dwordx4 a[160:163], v1, s[40:43], s50 offen nt\n"
        "buffer_load_dwordx4 a[164:167], v1, s[40:43], s50 offen offset:256 nt\n"
        "s_waitcnt vmcnt(8)\n"
        "s_call_b64 s[68:69], .Lst_pace_%=\n"
        "s_add_u32 s50, s48, s13\n"
        "buffer_load_dwordx4 a[168:171], v1, s[40:43], s50 offen nt\n"
        "buffer_load_dwordx4 a[172:175], v1, s[40:43], s50 offen offset:256 nt\n"
        "s_cmp_lg_u32 s74, 8\n"
        "s_cbranch_scc1 .Lst_noqk0_22_%=\n"
        "s_mov_b32 s63, s76\n"
        "s_mov_b64 vcc, 0\n"
        "s_call_b64 s[66:67], .Lst_qk0_%=\n"
        ".Lst_noqk0_22_%=:\n"
        "s_waitcnt vmcnt(8)\n"
        "s_call_b64 s[68:69], .Lst_pace_%=\n"
        "s_add_u32 s50, s48, s14\n"
        "buffer_load_dwordx4 a[176:179], v1, s[40:43], s50 offen nt\n"
        "buffer_load_dwordx4 a[180:183], v1, s[40:43], s50 offen offset:256 nt\n"
        "s_waitcnt vmcnt(8)\n"
        "s_call_b64 s[68:69], .Lst_pace_%=\n"
        "s_add_u32 s50, s48, s15\n"
        "buffer_load_dwordx4 a[184:187], v1, s[40:43], s50 offen nt\n"
        "buffer_load_dwordx4 a[188:191], v1, s[40:43], s50 offen offset:256 nt\n"
        "s_cmp_lg_u32 s74, 0\n"
        "s_cbranch_scc1 .Lst_noqk0_24_%=\n"
        "s_mov_b32 s63, s76\n"
        "s_mov_b64 vcc, 0\n"
        "s_call_b64 s[66:67], .Lst_qk0_%=\n"
        ".Lst_noqk0_24_%=:\n"
        "s_add_u32 s62, s62, 98304\n"
        "s_waitcnt vmcnt(8)\n"
        "s_call_b64 s[68:69], .Lst_pace_%=\n"
        "s_add_u32 s50, s48, s16\n"
        "buffer_load_dwordx4 a[192:195], v1, s[40:43], s50 offen nt\n"
        "buffer_load_dwordx4 a[196:199], v1, s[40:43], s50 offen offset:256 nt\n"
        "v_add_u32 v5, s54, v2\n"
        "ds_read_b128 v[32:35], v5 offset:0\n"
        "ds_read_b128 v[36:39], v5 offset:1024\n"
        "s_nop 1\n"
        "v_and_b32_dpp v16, v44, v40 row_newbcast:0 row_mask:0xf bank_mask:0xf\n"
        "v_and_b32_dpp v17, v44, v41 row_newbcast:0 row_mask:0xf bank_mask:0xf\n"
        "v_and_b32_dpp v18, v44, v42 row_newbcast:0 row_mask:0xf bank_mask:0xf\n"
        "v_and_b32_dpp v19, v44, v43 row_newbcast:0 row_mask:0xf bank_mask:0xf\n"
        "v_and_b32_dpp v20, v47, v40 row_newbcast:12 row_mask:0xf bank_mask:0xf\n"
        "v_and_b32_dpp v21, v47, v41 row_newbcast:12 row_mask:0xf bank_mask:0xf\n"
        "v_mfma_f32_16x16x32_bf16 v[24:27], v[16:19], a[120:123], 0\n"
        "v_and_b32_dpp v22, v47, v42 row_newbcast:12 row_mask:0xf bank_mask:0xf\n"
        "v_and_b32_dpp v23, v47, v43 row_newbcast:12 row_mask:0xf bank_mask:0xf\n"
        "v_mfma_f32_16x16x32_bf16 v[28:31], v[16:19], a[124:127], 0\n"
        "s_nop 1\n"
        "s_waitcnt lgkmcnt(1)\n"
        "v_mfma_f32_16x16x32_bf16 v[24:27], v[20:23], v[32:35], v[24:27]\n"
        "s_waitcnt lgkmcnt(0)\n"
        "v_mfma_f32_16x16x32_bf16 v[28:31], v[20:23], v[36:39], v[28:31]\n"
        "s_waitcnt vmcnt(8)\n"
        "s_call_b64 s[68:69], .Lst_pace_%=\n"
        "s_add_u32 s50, s48, s17\n"
        "buffer_load_dwordx4 a[200:203], v1, s[40:43], s50 offen nt\n"
        "buffer_load_dwordx4 a[204:207], v1, s[40:43], s50 offen offset:256 nt\n"
        "v_add_u32 v5, s54, v2\n"
        "ds_read_b128 v[32:35], v3 offset:0\n"
        "ds_read_b128 v[36:39], v3 offset:1024\n"
        "s_nop 1\n"
        "v_and_b32_dpp v16, v45, v40 row_newbcast:0 row_mask:0xf bank_mask:0xf\n"
        "v_and_b32_dpp v17, v45, v41 row_newbcast:0 row_mask:0xf bank_mask:0xf\n"
        "v_and_b32_dpp v18, v45, v42 row_newbcast:0 row_mask:0xf bank_mask:0xf\n"
        "v_and_b32_dpp v19, v45, v43 row_newbcast:0 row_mask:0xf bank_mask:0xf\n"
        "v_and_b32_dpp v20, v47, v40 row_newbcast:0 row_mask:0xf bank_mask:0xf\n"
        "v_and_b32_dpp v21, v47, v41 row_newbcast:0 row_mask:0xf bank_mask:0xf\n"
        "v_mfma_f32_16x16x32_bf16 v[24:27], v[16:19], a[128:131], v[24:27]\n"
        "v_and_b32_dpp v22, v47, v42 row_newbcast:0 row_mask:0xf bank_mask:0xf\n"
        "v_and_b32_dpp v23, v47, v43 row_newbcast:0 row_mask:0xf bank_mask:0xf\n"
        "v_mfma_f32_16x16x32_bf16 v[28:31], v[16:19], a[132:135], v[28:31]\n"
        "s_nop 1\n"
        "s_waitcnt lgkmcnt(1)\n"
        "v_mfma_f32_16x16x32_bf16 v[24:27], v[20:23], v[32:35], v[24:27]\n"
        "s_waitcnt lgkmcnt(0)\n"
        "v_mfma_f32_16x16x32_bf16 v[28:31], v[20:23], v[36:39], v[28:31]\n"
        "s_waitcnt vmcnt(8)\n"
        "s_call_b64 s[68:69], .Lst_pace_%=\n"
        "s_add_u32 s50, s48, s18\n"
        "buffer_load_dwordx4 a[208:211], v1, s[40:43], s50 offen nt\n"
        "buffer_load_dwordx4 a[212:215], v1, s[40:43], s50 offen offset:256 nt\n"
        "v_add_u32 v5, s54, v2\n"
        "ds_read_b128 v[32:35], v3 offset:2048\n"
        "ds_read_b128 v[36:39], v3 offset:3072\n"
        "s_nop 1\n"
        "v_and_b32_dpp v16, v46, v40 row_newbcast:0 row_mask:0xf bank_mask:0xf\n"
        "v_and_b32_dpp v17, v46, v41 row_newbcast:0 row_mask:0xf bank_mask:0xf\n"
        "v_and_b32_dpp v18, v46, v42 row_newbcast:0 row_mask:0xf bank_mask:0xf\n"
        "v_and_b32_dpp v19, v46, v43 row_newbcast:0 row_mask:0xf bank_mask:0xf\n"
        "v_and_b32_dpp v20, v46, v40 row_newbcast:4 row_mask:0xf bank_mask:0xf\n"
        "v_and_b32_dpp v21, v46, v41 row_newbcast:4 row_mask:0xf bank_mask:0xf\n"
        "v_mfma_f32_16x16x32_bf16 v[24:27], v[16:19], a[136:139], v[24:27]\n"
        "v_and_b32_dpp v22, v46, v42 row_newbcast:4 row_mask:0xf bank_mask:0xf\n"
        "v_and_b32_dpp v23, v46, v43 row_newbcast:4 row_mask:0xf bank_mask:0xf\n"
        "v_mfma_f32_16x16x32_bf16 v[28:31], v[16:19], a[140:143], v[28:31]\n"
        "s_nop 1\n"
        "s_waitcnt lgkmcnt(1)\n"
        "v_mfma_f32_16x16x32_bf16 v[24:27], v[20:23], v[32:35], v[24:27]\n"
        "s_waitcnt lgkmcnt(0)\n"
        "v_mfma_f32_16x16x32_bf16 v[28:31], v[20:23], v[36:39], v[28:31]\n"
        "s_waitcnt vmcnt(8)\n"
        "s_call_b64 s[68:69], .Lst_pace_%=\n"
        "s_add_u32 s50, s48, s19\n"
        "s_add_u32 m0, s53, 6144\n"
        "s_nop 0\n"
        "buffer_load_dwordx4 v1, s[40:43], s50 offen nt lds\n"
        "s_add_u32 m0, s53, 6912\n"
        "s_nop 0\n"
        "buffer_load_dwordx4 v1, s[40:43], s50 offen offset:256 nt lds\n"
        "v_add_u32 v5, s54, v2\n"
        "ds_read_b128 v[32:35], v3 offset:4096\n"
        "ds_read_b128 v[36:39], v3 offset:5120\n"
        "s_nop 1\n"
        "v_and_b32_dpp v16, v7, v40 row_newbcast:0 row_mask:0xf bank_mask:0xf\n"
        "v_and_b32_dpp v17, v7, v41 row_newbcast:0 row_mask:0xf bank_mask:0xf\n"
        "v_and_b32_dpp v18, v7, v42 row_newbcast:0 row_mask:0xf bank_mask:0xf\n"
        "v_and_b32_dpp v19, v7, v43 row_newbcast:0 row_mask:0xf bank_mask:0xf\n"
        "v_and_b32_dpp v20, v45, v40 row_newbcast:8 row_mask:0xf bank_mask:0xf\n"
        "v_and_b32_dpp v21, v45, v41 row_newbcast:8 row_mask:0xf bank_mask:0xf\n"
        "v_mfma_f32_16x16x32_bf16 v[24:27], v[16:19], a[144:147], v[24:27]\n"
        "v_and_b32_dpp v22, v45, v42 row_newbcast:8 row_mask:0xf bank_mask:0xf\n"
        "v_and_b32_dpp v23, v45, v43 row_newbcast:8 row_mask:0xf bank_mask:0xf\n"
        "v_mfma_f32_16x16x32_bf16 v[28:31], v[16:19], a[148:151], v[28:31]\n"
        "s_nop 1\n"
        "s_waitcnt lgkmcnt(1)\n"
        "v_mfma_f32_16x16x32_bf16 v[24:27], v[20:23], v[32:35], v[24:27]\n"
        "s_waitcnt lgkmcnt(0)\n"
        "v_mfma_f32_16x16x32_bf16 v[28:31], v[20:23], v[36:39], v[28:31]\n"
        "s_waitcnt vmcnt(8)\n"
        "s_call_b64 s[68:69], .Lst_pace_%=\n"
        "s_add_u32 s50, s48, s20\n"
        "buffer_load_dwordx4 a[216:219], v1, s[40:43], s50 offen nt\n"
        "buffer_load_dwordx4 a[220:223], v1, s[40:43], s50 offen offset:256 nt\n"
        "s_nop 1\n"
        "v_and_b32_dpp v16, v44, v40 row_newbcast:4 row_mask:0xf bank_mask:0xf\n"
        "v_and_b32_dpp v17, v44, v41 row_newbcast:4 row_mask:0xf bank_mask:0xf\n"
        "v_and_b32_dpp v18, v44, v42 row_newbcast:4 row_mask:0xf bank_mask:0xf\n"
        "v_and_b32_dpp v19, v44, v43 row_newbcast:4 row_mask:0xf bank_mask:0xf\n"
        "v_and_b32_dpp v20, v45, v40 row_newbcast:4 row_mask:0xf bank_mask:0xf\n"
        "v_and_b32_dpp v21, v45, v41 row_newbcast:4 row_mask:0xf bank_mask:0xf\n"
        "v_mfma_f32_16x16x32_bf16 v[24:27], v[16:19], a[152:155], v[24:27]\n"
        "v_and_b32_dpp v22, v45, v42 row_newbcast:4 row_mask:0xf bank_mask:0xf\n"
        "v_and_b32_dpp v23, v45, v43 row_newbcast:4 row_mask:0xf bank_mask:0xf\n"
        "v_mfma_f32_16x16x32_bf16 v[28:31], v[16:19], a[156:159], v[28:31]\n"
        "s_nop 1\n"
        "v_mfma_f32_16x16x32_bf16 v[24:27], v[20:23], a[160:163], v[24:27]\n"
        "v_mfma_f32_16x16x32_bf16 v[28:31], v[20:23], a[164:167], v[28:31]\n"
        "s_waitcnt vmcnt(8)\n"
        "s_call_b64 s[68:69], .Lst_pace_%=\n"
        "s_add_u32 s50, s48, s21\n"
        "buffer_load_dwordx4 a[224:227], v1, s[40:43], s50 offen nt\n"
        "buffer_load_dwordx4 a[228:231], v1, s[40:43], s50 offen offset:256 nt\n"
        "s_nop 1\n"
        "v_and_b32_dpp v16, v47, v40 row_newbcast:4 row_mask:0xf bank_mask:0xf\n"
        "v_and_b32_dpp v17, v47, v41 row_newbcast:4 row_mask:0xf bank_mask:0xf\n"
        "v_and_b32_dpp v18, v47, v42 row_newbcast:4 row_mask:0xf bank_mask:0xf\n"
        "v_and_b32_dpp v19, v47, v43 row_newbcast:4 row_mask:0xf bank_mask:0xf\n"
        "v_and_b32_dpp v20, v7, v40 row_newbcast:4 row_mask:0xf bank_mask:0xf\n"
        "v_and_b32_dpp v21, v7, v41 row_newbcast:4 row_mask:0xf bank_mask:0xf\n"
        "v_mfma_f32_16x16x32_bf16 v[24:27], v[16:19], a[168:171], v[24:27]\n"
        "v_and_b32_dpp v22, v7, v42 row_newbcast:4 row_mask:0xf bank_mask:0xf\n"
        "v_and_b32_dpp v23, v7, v43 row_newbcast:4 row_mask:0xf bank_mask:0xf\n"
        "v_mfma_f32_16x16x32_bf16 v[28:31], v[16:19], a[172:175], v[28:31]\n"
        "s_nop 1\n"
        "v_mfma_f32_16x16x32_bf16 v[24:27], v[20:23], a[176:179], v[24:27]\n"
        "v_mfma_f32_16x16x32_bf16 v[28:31], v[20:23], a[180:183], v[28:31]\n"
        "s_waitcnt vmcnt(8)\n"
        "s_call_b64 s[68:69], .Lst_pace_%=\n"
        "s_add_u32 s50, s49, s4\n"
        "s_add_u32 m0, s77, 0\n"
        "s_nop 0\n"
        "buffer_load_dwordx4 v0, s[36:39], s50 offen nt lds\n"
        "s_add_u32 m0, s77, 768\n"
        "s_nop 0\n"
        "buffer_load_dwordx4 v0, s[36:39], s50 offen offset:256 nt lds\n"
        "s_nop 1\n"
        "v_and_b32_dpp v16, v44, v40 row_newbcast:8 row_mask:0xf bank_mask:0xf\n"
        "v_and_b32_dpp v17, v44, v41 row_newbcast:8 row_mask:0xf bank_mask:0xf\n"
        "v_and_b32_dpp v18, v44, v42 row_newbcast:8 row_mask:0xf bank_mask:0xf\n"
        "v_and_b32_dpp v19, v44, v43 row_newbcast:8 row_mask:0xf bank_mask:0xf\n"
        "v_and_b32_dpp v20, v46, v40 row_newbcast:8 row_mask:0xf bank_mask:0xf\n"
        "v_and_b32_dpp v21, v46, v41 row_newbcast:8 row_mask:0xf bank_mask:0xf\n"
        "v_mfma_f32_16x16x32_bf16 v[24:27], v[16:19], a[184:187], v[24:27]\n"
        "v_and_b32_dpp v22, v46, v42 row_newbcast:8 row_mask:0xf bank_mask:0xf\n"
        "v_and_b32_dpp v23, v46, v43 row_newbcast:8 row_mask:0xf bank_mask:0xf\n"
        "v_mfma_f32_16x16x32_bf16 v[28:31], v[16:19], a[188:191], v[28:31]\n"
        "s_nop 1\n"
        "v_mfma_f32_16x16x32_bf16 v[24:27], v[20:23], a[192:195], v[24:27]\n"
        "v_mfma_f32_16x16x32_bf16 v[28:31], v[20:23], a[196:199], v[28:31]\n"
        "s_waitcnt vmcnt(8)\n"
        "s_mov_b64 exec, s[86:87]\n"
        "buffer_load_dwordx4 v[8:11], v2, s[44:47], s62 offen offset:0 sc1\n"
        "buffer_load_dwordx4 v[12:15], v2, s[44:47], s62 offen offset:1024 sc1\n"
        "s_mov_b64 exec, -1\n"
        "s_call_b64 s[68:69], .Lst_pace_%=\n"
        "s_add_u32 s50, s49, s5\n"
        "s_add_u32 m0, s77, 2048\n"
        "s_nop 0\n"
        "buffer_load_dwordx4 v0, s[36:39], s50 offen nt lds\n"
        "s_add_u32 m0, s77, 2816\n"
        "s_nop 0\n"
        "buffer_load_dwordx4 v0, s[36:39], s50 offen offset:256 nt lds\n"
        "s_nop 1\n"
        "v_and_b32_dpp v16, v47, v40 row_newbcast:8 row_mask:0xf bank_mask:0xf\n"
        "v_and_b32_dpp v17, v47, v41 row_newbcast:8 row_mask:0xf bank_mask:0xf\n"
        "v_and_b32_dpp v18, v47, v42 row_newbcast:8 row_mask:0xf bank_mask:0xf\n"
        "v_and_b32_dpp v19, v47, v43 row_newbcast:8 row_mask:0xf bank_mask:0xf\n"
        "v_and_b32_dpp v20, v7, v40 row_newbcast:8 row_mask:0xf bank_mask:0xf\n"
        "v_and_b32_dpp v21, v7, v41 row_newbcast:8 row_mask:0xf bank_mask:0xf\n"
        "v_mfma_f32_16x16x32_bf16 v[24:27], v[16:19], a[200:203], v[24:27]\n"
        "v_and_b32_dpp v22, v7, v42 row_newbcast:8 row_mask:0xf bank_mask:0xf\n"
        "v_and_b32_dpp v23, v7, v43 row_newbcast:8 row_mask:0xf bank_mask:0xf\n"
        "v_mfma_f32_16x16x32_bf16 v[28:31], v[16:19], a[204:207], v[28:31]\n"
        "s_nop 1\n"
        "v_mfma_f32_16x16x32_bf16 v[24:27], v[20:23], a[208:211], v[24:27]\n"
        "v_mfma_f32_16x16x32_bf16 v[28:31], v[20:23], a[212:215], v[28:31]\n"
        "s_waitcnt vmcnt(10)\n"
        "s_call_b64 s[68:69], .Lst_pace_%=\n"
        "s_add_u32 s50, s49, s6\n"
        "s_add_u32 m0, s77, 4096\n"
        "s_nop 0\n"
        "buffer_load_dwordx4 v0, s[36:39], s50 offen nt lds\n"
        "s_add_u32 m0, s77, 4864\n"
        "s_nop 0\n"
        "buffer_load_dwordx4 v0, s[36:39], s50 offen offset:256 nt lds\n"
        "v_add_u32 v5, s54, v2\n"
        "ds_read_b128 v[32:35], v3 offset:6144\n"
        "ds_read_b128 v[36:39], v3 offset:7168\n"
        "s_nop 1\n"
        "v_and_b32_dpp v16, v44, v40 row_newbcast:12 row_mask:0xf bank_mask:0xf\n"
        "v_and_b32_dpp v17, v44, v41 row_newbcast:12 row_mask:0xf bank_mask:0xf\n"
        "v_and_b32_dpp v18, v44, v42 row_newbcast:12 row_mask:0xf bank_mask:0xf\n"
        "v_and_b32_dpp v19, v44, v43 row_newbcast:12 row_mask:0xf bank_mask:0xf\n"
        "s_nop 1\n"
        "s_waitcnt lgkmcnt(1)\n"
        "v_mfma_f32_16x16x32_bf16 v[24:27], v[16:19], v[32:35], v[24:27]\n"
        "s_waitcnt lgkmcnt(0)\n"
        "v_mfma_f32_16x16x32_bf16 v[28:31], v[16:19], v[36:39], v[28:31]\n"
        "s_waitcnt vmcnt(10)\n"
        "s_mov_b64 exec, s[86:87]\n"
        "buffer_load_dwordx4 v[8:11], v2, s[44:47], s62 offen offset:0 sc1\n"
        "buffer_load_dwordx4 v[12:15], v2, s[44:47], s62 offen offset:1024 sc1\n"
        "s_mov_b64 exec, -1\n"
        "s_nop 1\n"
        "v_and_b32_dpp v16, v45, v40 row_newbcast:12 row_mask:0xf bank_mask:0xf\n"
        "v_and_b32_dpp v17, v45, v41 row_newbcast:12 row_mask:0xf bank_mask:0xf\n"
        "v_and_b32_dpp v18, v45, v42 row_newbcast:12 row_mask:0xf bank_mask:0xf\n"
        "v_and_b32_dpp v19, v45, v43 row_newbcast:12 row_mask:0xf bank_mask:0xf\n"
        "s_nop 1\n"
        "v_mfma_f32_16x16x32_bf16 v[24:27], v[16:19], a[216:219], v[24:27]\n"
        "v_mfma_f32_16x16x32_bf16 v[28:31], v[16:19], a[220:223], v[28:31]\n"
        "s_waitcnt vmcnt(10)\n"
        "s_nop 1\n"
        "v_and_b32_dpp v16, v46, v40 row_newbcast:12 row_mask:0xf bank_mask:0xf\n"
        "v_and_b32_dpp v17, v46, v41 row_newbcast:12 row_mask:0xf bank_mask:0xf\n"
        "v_and_b32_dpp v18, v46, v42 row_newbcast:12 row_mask:0xf bank_mask:0xf\n"
        "v_and_b32_dpp v19, v46, v43 row_newbcast:12 row_mask:0xf bank_mask:0xf\n"
        "s_nop 1\n"
        "v_mfma_f32_16x16x32_bf16 v[24:27], v[16:19], a[224:227], v[24:27]\n"
        "v_mfma_f32_16x16x32_bf16 v[28:31], v[16:19], a[228:231], v[28:31]\n"
        "s_mul_i32 s64, s51, 143360\n"
        "s_add_u32 s64, s64, s56\n"
        "s_call_b64 s[66:67], .Lst_epi_%=\n"
        "s_cmp_eq_u32 s51, 0\n"
        "s_cbranch_scc1 .Lst_nop2a_%=\n"
        "s_waitcnt vmcnt(6)\n"
        "v_max3_u32 v16, v8, v9, v10\n"
        "v_max3_u32 v17, v11, v12, v13\n"
        "v_max3_u32 v16, v16, v14, v15\n"
        "v_max_u32 v16, v16, v17\n"
        "v_cmp_eq_u32 vcc, -1, v16\n"
        "s_cbranch_vccz .Lst_gq2_0_%=\n"
        "s_waitcnt vmcnt(0)\n"
        "v_max3_u32 v16, v8, v9, v10\n"
        "v_max3_u32 v17, v11, v12, v13\n"
        "v_max3_u32 v16, v16, v14, v15\n"
        "v_max_u32 v16, v16, v17\n"
        "v_cmp_eq_u32 vcc, -1, v16\n"
        "s_cbranch_vccz .Lst_gq2_0_%=\n"
        "s_call_b64 s[66:67], .Lst_spinq_%=\n"
        ".Lst_gq2_0_%=:\n"
        "s_mov_b32 s63, s75\n"
        "s_mov_b64 vcc, -1\n"
        "s_call_b64 s[66:67], .Lst_qk1_%=\n"
        "s_branch .Lst_p2a_%=\n"
        ".Lst_nop2a_%=:\n"
        "s_call_b64 s[68:69], .Lst_pace_%=\n"
        "s_add_u32 s50, s49, s7\n"
        "buffer_load_dwordx4 a[232:235], v0, s[36:39], s50 offen nt\n"
        "buffer_load_dwordx4 a[236:239], v0, s[36:39], s50 offen offset:256 nt\n"
        "s_call_b64 s[68:69], .Lst_pace_%=\n"
        "s_add_u32 s50, s49, s8\n"
        "buffer_load_dwordx4 a[240:243], v0, s[36:39], s50 offen nt\n"
        "buffer_load_dwordx4 a[244:247], v0, s[36:39], s50 offen offset:256 nt\n"
        "s_call_b64 s[68:69], .Lst_pace_%=\n"
        "s_add_u32 s50, s49, s9\n"
        "buffer_load_dwordx4 a[248:251], v0, s[36:39], s50 offen nt\n"
        "buffer_load_dwordx4 a[252:255], v0, s[36:39], s50 offen offset:256 nt\n"
        ".Lst_p2a_%=:\n"
        "s_call_b64 s[68:69], .Lst_pace_%=\n"
        "s_add_u32 s50, s49, s10\n"
        "buffer_load_dwordx4 v[48:51], v0, s[36:39], s50 offen nt\n"
        "buffer_load_dwordx4 v[52:55], v0, s[36:39], s50 offen offset:256 nt\n"
        "s_barrier\n"
        "s_cmp_eq_u32 s51, 0\n"
        "s_cbranch_scc1 .Lst_nop2b_%=\n"
        "s_add_u32 s64, s64, 430080\n"
        "s_call_b64 s[66:67], .Lst_pv1_%=\n"
        "s_branch .Lst_p2b_%=\n"
        ".Lst_nop2b_%=:\n"
        "s_call_b64 s[68:69], .Lst_pace_%=\n"
        "s_add_u32 s50, s49, s11\n"
        "buffer_load_dwordx4 v[56:59], v0, s[36:39], s50 offen nt\n"
        "buffer_load_dwordx4 v[60:63], v0, s[36:39], s50 offen offset:256 nt\n"
        "s_call_b64 s[68:69], .Lst_pace_%=\n"
        "s_add_u32 s50, s49, s12\n"
        "buffer_load_dwordx4 v[64:67], v0, s[36:39], s50 offen nt\n"
        "buffer_load_dwordx4 v[68:71], v0, s[36:39], s50 offen offset:256 nt\n"
        ".Lst_p2b_%=:\n"
        "s_add_u32 s51, s51, 1\n"
        "s_mov_b32 s48, s49\n"
        "s_cmp_ge_u32 s51, 3\n"
        "s_cselect_b32 s59, 0, 0x2000000\n"
        "s_add_u32 s49, s49, s59\n"
        "s_mov_b32 s59, s75\n"
        "s_mov_b32 s75, s76\n"
        "s_mov_b32 s76, s77\n"
        "s_mov_b32 s77, s59\n"
        "s_mul_i32 s62, s51, 32768\n"
        "s_add_u32 s62, s62, s55\n"
        "s_waitcnt vmcnt(20)\n"
        "s_call_b64 s[68:69], .Lst_pace_%=\n"
        "s_add_u32 s50, s48, s13\n"
        "buffer_load_dwordx4 v[72:75], v0, s[36:39], s50 offen nt\n"
        "buffer_load_dwordx4 v[76:79], v0, s[36:39], s50 offen offset:256 nt\n"
        "s_waitcnt vmcnt(18)\n"
        "s_call_b64 s[68:69], .Lst_pace_%=\n"
        "s_add_u32 s50, s48, s14\n"
        "buffer_load_dwordx4 v[80:83], v0, s[36:39], s50 offen nt\n"
        "buffer_load_dwordx4 v[84:87], v0, s[36:39], s50 offen offset:256 nt\n"
        "s_waitcnt vmcnt(18)\n"
        "s_call_b64 s[68:69], .Lst_pace_%=\n"
        "s_add_u32 s50, s48, s15\n"
        "buffer_load_dwordx4 v[88:91], v0, s[36:39], s50 offen nt\n"
        "buffer_load_dwordx4 v[92:95], v0, s[36:39], s50 offen offset:256 nt\n"
        "s_waitcnt vmcnt(16)\n"
        "s_call_b64 s[68:69], .Lst_pace_%=\n"
        "s_add_u32 s50, s48, s16\n"
        "buffer_load_dwordx4 v[96:99], v0, s[36:39], s50 offen nt\n"
        "buffer_load_dwordx4 v[100:103], v0, s[36:39], s50 offen offset:256 nt\n"
        "s_waitcnt vmcnt(16)\n"
        "s_call_b64 s[68:69], .Lst_pace_%=\n"
        "s_cmp_eq_u32 s52, 0\n"
        "s_cbranch_scc1 .Lst_noopt2_%=\n"
        "s_add_u32 s50, s48, s89\n"
        "s_add_u32 m0, s54, 12288\n"
        "s_nop 0\n"
        "buffer_load_dwordx4 v0, s[36:39], s50 offen nt lds\n"
        "s_add_u32 m0, s54, 13056\n"
        "s_nop 0\n"
        "buffer_load_dwordx4 v0, s[36:39], s50 offen offset:256 nt lds\n"
        ".Lst_noopt2_%=:\n"
        "s_add_u32 s50, s48, s17\n"
        "buffer_load_dwordx4 v[104:107], v0, s[36:39], s50 offen nt\n"
        "buffer_load_dwordx4 v[108:111], v0, s[36:39], s50 offen offset:256 nt\n"
        "s_waitcnt vmcnt(16)\n"
        "s_call_b64 s[68:69], .Lst_pace_%=\n"
        "s_add_u32 s50, s48, s18\n"
        "buffer_load_dwordx4 v[112:115], v0, s[36:39], s50 offen nt\n"
        "buffer_load_dwordx4 v[116:119], v0, s[36:39], s50 offen offset:256 nt\n"
        "s_waitcnt vmcnt(16)\n"
        "s_call_b64 s[68:69], .Lst_pace_%=\n"
        "s_add_u32 s50, s48, s19\n"
        "buffer_load_dwordx4 v[120:123], v0, s[36:39], s50 offen nt\n"
        "buffer_load_dwordx4 v[124:127], v0, s[36:39], s50 offen offset:256 nt\n"
        "s_waitcnt vmcnt(16)\n"
        "s_call_b64 s[68:69], .Lst_pace_%=\n"
        "s_add_u32 s50, s48, s20\n"
        "buffer_load_dwordx4 v[128:131], v0, s[36:39], s50 offen nt\n"
        "buffer_load_dwordx4 v[132:135], v0, s[36:39], s50 offen offset:256 nt\n"
        "s_waitcnt vmcnt(16)\n"
        "s_call_b64 s[68:69], .Lst_pace_%=\n"
        "s_add_u32 s50, s48, s21\n"
        "buffer_load_dwordx4 v[136:139], v0, s[36:39], s50 offen nt\n"
        "buffer_load_dwordx4 v[140:143], v0, s[36:39], s50 offen offset:256 nt\n"
        "s_waitcnt vmcnt(16)\n"
        "s_mov_b64 exec, s[86:87]\n"
        "buffer_load_dwordx4 v[8:11], v2, s[44:47], s62 offen offset:0 sc1\n"
        "buffer_load_dwordx4 v[12:15], v2, s[44:47], s62 offen offset:1024 sc1\n"
        "buffer_load_dwordx4 v[32:35], v2, s[44:47], s62 offen offset:128 sc1\n"
        "buffer_load_dwordx4 v[36:39], v2, s[44:47], s62 offen offset:1152 sc1\n"
        "s_mov_b64 exec, -1\n"
        "s_call_b64 s[68:69], .Lst_pace_%=\n"
        "s_cmp_eq_u32 s52, 0\n"
        "s_cbranch_scc1 .Lst_noopt3_%=\n"
        "s_add_u32 s50, s48, s89\n"
        "s_add_u32 m0, s54, 8192\n"
        "s_nop 0\n"
        "buffer_load_dwordx4 v1, s[40:43], s50 offen nt lds\n"
        "s_add_u32 m0, s54, 8960\n"
        "s_nop 0\n"
        "buffer_load_dwordx4 v1, s[40:43], s50 offen offset:256 nt lds\n"
        ".Lst_noopt3_%=:\n"
        "s_add_u32 s50, s48, s4\n"
        "s_add_u32 m0, s53, 8192\n"
        "s_nop 0\n"
        "buffer_load_dwordx4 v1, s[40:43], s50 offen nt lds\n"
        "s_add_u32 m0, s53, 8960\n"
        "s_nop 0\n"
        "buffer_load_dwordx4 v1, s[40:43], s50 offen offset:256 nt lds\n"
        "s_waitcnt vmcnt(20)\n"
        "s_call_b64 s[68:69], .Lst_pace_%=\n"
        "s_add_u32 s50, s48, s5\n"
        "s_add_u32 m0, s53, 10240\n"
        "s_nop 0\n"
        "buffer_load_dwordx4 v1, s[40:43], s50 offen nt lds\n"
        "s_add_u32 m0, s53, 11008\n"
        "s_nop 0\n"
        "buffer_load_dwordx4 v1, s[40:43], s50 offen offset:256 nt lds\n"
        "s_waitcnt vmcnt(20)\n"
        "s_call_b64 s[68:69], .Lst_pace_%=\n"
        "s_add_u32 s50, s48, s6\n"
        "s_add_u32 m0, s53, 12288\n"
        "s_nop 0\n"
        "buffer_load_dwordx4 v1, s[40:43], s50 offen nt lds\n"
        "s_add_u32 m0, s53, 13056\n"
        "s_nop 0\n"
        "buffer_load_dwordx4 v1, s[40:43], s50 offen offset:256 nt lds\n"
        "s_waitcnt vmcnt(20)\n"
        "s_call_b64 s[68:69], .Lst_pace_%=\n"
        "s_add_u32 s50, s48, s7\n"
        "buffer_load_dwordx4 v[144:147], v1, s[40:43], s50 offen nt\n"
        "buffer_load_dwordx4 v[148:151], v1, s[40:43], s50 offen offset:256 nt\n"
        "s_barrier\n"
        "s_waitcnt vmcnt(12)\n"
        "s_call_b64 s[68:69], .Lst_pace_%=\n"
        "s_add_u32 s50, s48, s8\n"
        "buffer_load_dwordx4 v[152:155], v1, s[40:43], s50 offen nt\n"
        "buffer_load_dwordx4 v[156:159], v1, s[40:43], s50 offen offset:256 nt\n"
        "s_waitcnt vmcnt(10)\n"
        "s_call_b64 s[66:67], .Lst_chkq1_%=\n"
        "s_waitcnt vmcnt(8)\n"
        "s_call_b64 s[68:69], .Lst_pace_%=\n"
        "s_add_u32 s50, s48, s9\n"
        "buffer_load_dwordx4 v[160:163], v1, s[40:43], s50 offen nt\n"
        "buffer_load_dwordx4 v[164:167], v1, s[40:43], s50 offen offset:256 nt\n"
        "s_cmp_lg_u32 s74, 24\n"
        "s_cbranch_scc1 .Lst_noqk1_18_%=\n"
        "s_mov_b32 s63, s76\n"
        "s_mov_b64 vcc, 0\n"
        "s_call_b64 s[66:67], .Lst_qk1_%=\n"
        ".Lst_noqk1_18_%=:\n"
        "s_waitcnt vmcnt(8)\n"
        "s_call_b64 s[68:69], .Lst_pace_%=\n"
        "s_add_u32 s50, s48, s10\n"
        "buffer_load_dwordx4 v[168:171], v1, s[40:43], s50 offen nt\n"
        "buffer_load_dwordx4 v[172:175], v1, s[40:43], s50 offen offset:256 nt\n"
        "s_waitcnt vmcnt(8)\n"
        "s_call_b64 s[68:69], .Lst_pace_%=\n"
        "s_add_u32 s50, s48, s11\n"
        "buffer_load_dwordx4 v[176:179], v1, s[40:43], s50 offen nt\n"
        "buffer_load_dwordx4 v[180:183], v1, s[40:43], s50 offen offset:256 nt\n"
        "s_cmp_lg_u32 s74, 16\n"
        "s_cbranch_scc1 .Lst_noqk1_20_%=\n"
        "s_mov_b32 s63, s76\n"
        "s_mov_b64 vcc, 0\n"
        "s_call_b64 s[66:67], .Lst_qk1_%=\n"
        ".Lst_noqk1_20_%=:\n"
        "s_waitcnt vmcnt(8)\n"
        "s_call_b64 s[68:69], .Lst_pace_%=\n"
        "s_add_u32 s50, s48, s12\n"
        "buffer_load_dwordx4 v[184:187], v1, s[40:43], s50 offen nt\n"
        "buffer_load_dwordx4 v[188:191], v1, s[40:43], s50 offen offset:256 nt\n"
        "s_waitcnt vmcnt(8)\n"
        "s_call_b64 s[68:69], .Lst_pace_%=\n"
        "s_add_u32 s50, s48, s13\n"
        "buffer_load_dwordx4 v[192:195], v1, s[40:43], s50 offen nt\n"
        "buffer_load_dwordx4 v[196:199], v1, s[40:43], s50 offen offset:256 nt\n"
        "s_cmp_lg_u32 s74, 8\n"
        "s_cbranch_scc1 .Lst_noqk1_22_%=\n"
        "s_mov_b32 s63, s76\n"
        "s_mov_b64 vcc, 0\n"
        "s_call_b64 s[66:67], .Lst_qk1_%=\n"
        ".Lst_noqk1_22_%=:\n"
        "s_waitcnt vmcnt(8)\n"
        "s_call_b64 s[68:69], .Lst_pace_%=\n"
        "s_add_u32 s50, s48, s14\n"
        "buffer_load_dwordx4 v[200:203], v1, s[40:43], s50 offen nt\n"
        "buffer_load_dwordx4 v[204:207], v1, s[40:43], s50 offen offset:256 nt\n"
        "s_waitcnt vmcnt(8)\n"
        "s_call_b64 s[68:69], .Lst_pace_%=\n"
        "s_add_u32 s50, s48, s15\n"
        "buffer_load_dwordx4 v[208:211], v1, s[40:43], s50 offen nt\n"
        "buffer_load_dwordx4 v[212:215], v1, s[40:43], s50 offen offset:256 nt\n"
        "s_cmp_lg_u32 s74, 0\n"
        "s_cbranch_scc1 .Lst_noqk1_24_%=\n"
        "s_mov_b32 s63, s76\n"
        "s_mov_b64 vcc, 0\n"
        "s_call_b64 s[66:67], .Lst_qk1_%=\n"
        ".Lst_noqk1_24_%=:\n"
        "s_add_u32 s62, s62, 98304\n"
        "s_waitcnt vmcnt(8)\n"
        "s_call_b64 s[68:69], .Lst_pace_%=\n"
        "s_add_u32 s50, s48, s16\n"
        "buffer_load_dwordx4 v[216:219], v1, s[40:43], s50 offen nt\n"
        "buffer_load_dwordx4 v[220:223], v1, s[40:43], s50 offen offset:256 nt\n"
        "v_add_u32 v5, s54, v2\n"
        "ds_read_b128 v[32:35], v5 offset:8192\n"
        "ds_read_b128 v[36:39], v5 offset:9216\n"
        "s_nop 1\n"
        "v_and_b32_dpp v16, v44, v40 row_newbcast:0 row_mask:0xf bank_mask:0xf\n"
        "v_and_b32_dpp v17, v44, v41 row_newbcast:0 row_mask:0xf bank_mask:0xf\n"
        "v_and_b32_dpp v18, v44, v42 row_newbcast:0 row_mask:0xf bank_mask:0xf\n"
        "v_and_b32_dpp v19, v44, v43 row_newbcast:0 row_mask:0xf bank_mask:0xf\n"
        "v_and_b32_dpp v20, v47, v40 row_newbcast:12 row_mask:0xf bank_mask:0xf\n"
        "v_and_b32_dpp v21, v47, v41 row_newbcast:12 row_mask:0xf bank_mask:0xf\n"
        "v_mfma_f32_16x16x32_bf16 v[24:27], v[16:19], v[144:147], 0\n"
        "v_and_b32_dpp v22, v47, v42 row_newbcast:12 row_mask:0xf bank_mask:0xf\n"
        "v_and_b32_dpp v23, v47, v43 row_newbcast:12 row_mask:0xf bank_mask:0xf\n"
        "v_mfma_f32_16x16x32_bf16 v[28:31], v[16:19], v[148:151], 0\n"
        "s_nop 1\n"
        "s_waitcnt lgkmcnt(1)\n"
        "v_mfma_f32_16x16x32_bf16 v[24:27], v[20:23], v[32:35], v[24:27]\n"
        "s_waitcnt lgkmcnt(0)\n"
        "v_mfma_f32_16x16x32_bf16 v[28:31], v[20:23], v[36:39], v[28:31]\n"
        "s_waitcnt vmcnt(8)\n"
        "s_call_b64 s[68:69], .Lst_pace_%=\n"
        "s_add_u32 s50, s48, s17\n"
        "buffer_load_dwordx4 v[224:227], v1, s[40:43], s50 offen nt\n"
        "buffer_load_dwordx4 v[228:231], v1, s[40:43], s50 offen offset:256 nt\n"
        "v_add_u32 v5, s54, v2\n"
        "ds_read_b128 v[32:35], v3 offset:8192\n"
        "ds_read_b128 v[36:39], v3 offset:9216\n"
        "s_nop 1\n"
        "v_and_b32_dpp v16, v45, v40 row_newbcast:0 row_mask:0xf bank_mask:0xf\n"
        "v_and_b32_dpp v17, v45, v41 row_newbcast:0 row_mask:0xf bank_mask:0xf\n"
        "v_and_b32_dpp v18, v45, v42 row_newbcast:0 row_mask:0xf bank_mask:0xf\n"
        "v_and_b32_dpp v19, v45, v43 row_newbcast:0 row_mask:0xf bank_mask:0xf\n"
        "v_and_b32_dpp v20, v47, v40 row_newbcast:0 row_mask:0xf bank_mask:0xf\n"
        "v_and_b32_dpp v21, v47, v41 row_newbcast:0 row_mask:0xf bank_mask:0xf\n"
        "v_mfma_f32_16x16x32_bf16 v[24:27], v[16:19], v[152:155], v[24:27]\n"
        "v_and_b32_dpp v22, v47, v42 row_newbcast:0 row_mask:0xf bank_mask:0xf\n"
        "v_and_b32_dpp v23, v47, v43 row_newbcast:0 row_mask:0xf bank_mask:0xf\n"
        "v_mfma_f32_16x16x32_bf16 v[28:31], v[16:19], v[156:159], v[28:31]\n"
        "s_nop 1\n"
        "s_waitcnt lgkmcnt(1)\n"
        "v_mfma_f32_16x16x32_bf16 v[24:27], v[20:23], v[32:35], v[24:27]\n"
        "s_waitcnt lgkmcnt(0)\n"
        "v_mfma_f32_16x16x32_bf16 v[28:31], v[20:23], v[36:39], v[28:31]\n"
        "s_waitcnt vmcnt(8)\n"
        "s_call_b64 s[68:69], .Lst_pace_%=\n"
        "s_add_u32 s50, s48, s18\n"
        "buffer_load_dwordx4 v[232:235], v1, s[40:43], s50 offen nt\n"
        "buffer_load_dwordx4 v[236:239], v1, s[40:43], s50 offen offset:256 nt\n"
        "v_add_u32 v5, s54, v2\n"
        "ds_read_b128 v[32:35], v3 offset:10240\n"
        "ds_read_b128 v[36:39], v3 offset:11264\n"
        "s_nop 1\n"
        "v_and_b32_dpp v16, v46, v40 row_newbcast:0 row_mask:0xf bank_mask:0xf\n"
        "v_and_b32_dpp v17, v46, v41 row_newbcast:0 row_mask:0xf bank_mask:0xf\n"
        "v_and_b32_dpp v18, v46, v42 row_newbcast:0 row_mask:0xf bank_mask:0xf\n"
        "v_and_b32_dpp v19, v46, v43 row_newbcast:0 row_mask:0xf bank_mask:0xf\n"
        "v_and_b32_dpp v20, v46, v40 row_newbcast:4 row_mask:0xf bank_mask:0xf\n"
        "v_and_b32_dpp v21, v46, v41 row_newbcast:4 row_mask:0xf bank_mask:0xf\n"
        "v_mfma_f32_16x16x32_bf16 v[24:27], v[16:19], v[160:163], v[24:27]\n"
        "v_and_b32_dpp v22, v46, v42 row_newbcast:4 row_mask:0xf bank_mask:0xf\n"
        "v_and_b32_dpp v23, v46, v43 row_newbcast:4 row_mask:0xf bank_mask:0xf\n"
        "v_mfma_f32_16x16x32_bf16 v[28:31], v[16:19], v[164:167], v[28:31]\n"
        "s_nop 1\n"
        "s_waitcnt lgkmcnt(1)\n"
        "v_mfma_f32_16x16x32_bf16 v[24:27], v[20:23], v[32:35], v[24:27]\n"
        "s_waitcnt lgkmcnt(0)\n"
        "v_mfma_f32_16x16x32_bf16 v[28:31], v[20:23], v[36:39], v[28:31]\n"
        "s_waitcnt vmcnt(8)\n"
        "s_call_b64 s[68:69], .Lst_pace_%=\n"
        "s_add_u32 s50, s48, s19\n"
        "s_add_u32 m0, s53, 14336\n"
        "s_nop 0\n"
        "buffer_load_dwordx4 v1, s[40:43], s50 offen nt lds\n"
        "s_add_u32 m0, s53, 15104\n"
        "s_nop 0\n"
        "buffer_load_dwordx4 v1, s[40:43], s50 offen offset:256 nt lds\n"
        "v_add_u32 v5, s54, v2\n"
        "ds_read_b128 v[32:35], v3 offset:12288\n"
        "ds_read_b128 v[36:39], v3 offset:13312\n"
        "s_nop 1\n"
        "v_and_b32_dpp v16, v7, v40 row_newbcast:0 row_mask:0xf bank_mask:0xf\n"
        "v_and_b32_dpp v17, v7, v41 row_newbcast:0 row_mask:0xf bank_mask:0xf\n"
        "v_and_b32_dpp v18, v7, v42 row_newbcast:0 row_mask:0xf bank_mask:0xf\n"
        "v_and_b32_dpp v19, v7, v43 row_newbcast:0 row_mask:0xf bank_mask:0xf\n"
        "v_and_b32_dpp v20, v45, v40 row_newbcast:8 row_mask:0xf bank_mask:0xf\n"
        "v_and_b32_dpp v21, v45, v41 row_newbcast:8 row_mask:0xf bank_mask:0xf\n"
        "v_mfma_f32_16x16x32_bf16 v[24:27], v[16:19], v[168:171], v[24:27]\n"
        "v_and_b32_dpp v22, v45, v42 row_newbcast:8 row_mask:0xf bank_mask:0xf\n"
        "v_and_b32_dpp v23, v45, v43 row_newbcast:8 row_mask:0xf bank_mask:0xf\n"
        "v_mfma_f32_16x16x32_bf16 v[28:31], v[16:19], v[172:175], v[28:31]\n"
        "s_nop 1\n"
        "s_waitcnt lgkmcnt(1)\n"
        "v_mfma_f32_16x16x32_bf16 v[24:27], v[20:23], v[32:35], v[24:27]\n"
        "s_waitcnt lgkmcnt(0)\n"
        "v_mfma_f32_16x16x32_bf16 v[28:31], v[20:23], v[36:39], v[28:31]\n"
        "s_waitcnt vmcnt(8)\n"
        "s_call_b64 s[68:69], .Lst_pace_%=\n"
        "s_add_u32 s50, s48, s20\n"
        "buffer_load_dwordx4 v[240:243], v1, s[40:43], s50 offen nt\n"
        "buffer_load_dwordx4 v[244:247], v1, s[40:43], s50 offen offset:256 nt\n"
        "s_nop 1\n"
        "v_and_b32_dpp v16, v44, v40 row_newbcast:4 row_mask:0xf bank_mask:0xf\n"
        "v_and_b32_dpp v17, v44, v41 row_newbcast:4 row_mask:0xf bank_mask:0xf\n"
        "v_and_b32_dpp v18, v44, v42 row_newbcast:4 row_mask:0xf bank_mask:0xf\n"
        "v_and_b32_dpp v19, v44, v43 row_newbcast:4 row_mask:0xf bank_mask:0xf\n"
        "v_and_b32_dpp v20, v45, v40 row_newbcast:4 row_mask:0xf bank_mask:0xf\n"
        "v_and_b32_dpp v21, v45, v41 row_newbcast:4 row_mask:0xf bank_mask:0xf\n"
        "v_mfma_f32_16x16x32_bf16 v[24:27], v[16:19], v[176:179], v[24:27]\n"
        "v_and_b32_dpp v22, v45, v42 row_newbcast:4 row_mask:0xf bank_mask:0xf\n"
        "v_and_b32_dpp v23, v45, v43 row_newbcast:4 row_mask:0xf bank_mask:0xf\n"
        "v_mfma_f32_16x16x32_bf16 v[28:31], v[16:19], v[180:183], v[28:31]\n"
        "s_nop 1\n"
        "v_mfma_f32_16x16x32_bf16 v[24:27], v[20:23], v[184:187], v[24:27]\n"
        "v_mfma_f32_16x16x32_bf16 v[28:31], v[20:23], v[188:191], v[28:31]\n"
        "s_waitcnt vmcnt(8)\n"
        "s_call_b64 s[68:69], .Lst_pace_%=\n"
        "s_add_u32 s50, s48, s21\n"
        "buffer_load_dwordx4 v[248:251], v1, s[40:43], s50 offen nt\n"
        "buffer_load_dwordx4 v[252:255], v1, s[40:43], s50 offen offset:256 nt\n"
        "s_nop 1\n"
        "v_and_b32_dpp v16, v47, v40 row_newbcast:4 row_mask:0xf bank_mask:0xf\n"
        "v_and_b32_dpp v17, v47, v41 row_newbcast:4 row_mask:0xf bank_mask:0xf\n"
        "v_and_b32_dpp v18, v47, v42 row_newbcast:4 row_mask:0xf bank_mask:0xf\n"
        "v_and_b32_dpp v19, v47, v43 row_newbcast:4 row_mask:0xf bank_mask:0xf\n"
        "v_and_b32_dpp v20, v7, v40 row_newbcast:4 row_mask:0xf bank_mask:0xf\n"
        "v_and_b32_dpp v21, v7, v41 row_newbcast:4 row_mask:0xf bank_mask:0xf\n"
        "v_mfma_f32_16x16x32_bf16 v[24:27], v[16:19], v[192:195], v[24:27]\n"
        "v_and_b32_dpp v22, v7, v42 row_newbcast:4 row_mask:0xf bank_mask:0xf\n"
        "v_and_b32_dpp v23, v7, v43 row_newbcast:4 row_mask:0xf bank_mask:0xf\n"
        "v_mfma_f32_16x16x32_bf16 v[28:31], v[16:19], v[196:199], v[28:31]\n"
        "s_nop 1\n"
        "v_mfma_f32_16x16x32_bf16 v[24:27], v[20:23], v[200:203], v[24:27]\n"
        "v_mfma_f32_16x16x32_bf16 v[28:31], v[20:23], v[204:207], v[28:31]\n"
        "s_waitcnt vmcnt(8)\n"
        "s_cmp_eq_u32 s51, 3\n"
        "s_cselect_b32 s60, 0x40000000, 0\n"
        "v_or_b32 v0, s60, v0\n"
        "s_lshr_b32 s60, s60, 8\n"
        "s_sub_u32 s84, s84, s60\n"
        "s_add_u32 s85, s85, s60\n"
        "s_sub_u32 s88, s88, s60\n"
        "s_call_b64 s[68:69], .Lst_pace_%=\n"
        "s_add_u32 s50, s49, s4\n"
        "s_add_u32 m0, s77, 0\n"
        "s_nop 0\n"
        "buffer_load_dwordx4 v0, s[36:39], s50 offen nt lds\n"
        "s_add_u32 m0, s77, 768\n"
        "s_nop 0\n"
        "buffer_load_dwordx4 v0, s[36:39], s50 offen offset:256 nt lds\n"
        "s_nop 1\n"
        "v_and_b32_dpp v16, v44, v40 row_newbcast:8 row_mask:0xf bank_mask:0xf\n"
        "v_and_b32_dpp v17, v44, v41 row_newbcast:8 row_mask:0xf bank_mask:0xf\n"
        "v_and_b32_dpp v18, v44, v42 row_newbcast:8 row_mask:0xf bank_mask:0xf\n"
        "v_and_b32_dpp v19, v44, v43 row_newbcast:8 row_mask:0xf bank_mask:0xf\n"
        "v_and_b32_dpp v20, v46, v40 row_newbcast:8 row_mask:0xf bank_mask:0xf\n"
        "v_and_b32_dpp v21, v46, v41 row_newbcast:8 row_mask:0xf bank_mask:0xf\n"
        "v_mfma_f32_16x16x32_bf16 v[24:27], v[16:19], v[208:211], v[24:27]\n"
        "v_and_b32_dpp v22, v46, v42 row_newbcast:8 row_mask:0xf bank_mask:0xf\n"
        "v_and_b32_dpp v23, v46, v43 row_newbcast:8 row_mask:0xf bank_mask:0xf\n"
        "v_mfma_f32_16x16x32_bf16 v[28:31], v[16:19], v[212:215], v[28:31]\n"
        "s_nop 1\n"
        "v_mfma_f32_16x16x32_bf16 v[24:27], v[20:23], v[216:219], v[24:27]\n"
        "v_mfma_f32_16x16x32_bf16 v[28:31], v[20:23], v[220:223], v[28:31]\n"
        "s_waitcnt vmcnt(8)\n"
        "s_mov_b64 exec, s[86:87]\n"
        "buffer_load_dwordx4 v[8:11], v2, s[44:47], s62 offen offset:0 sc1\n"
        "buffer_load_dwordx4 v[12:15], v2, s[44:47], s62 offen offset:1024 sc1\n"
        "s_mov_b64 exec, -1\n"
        "s_call_b64 s[68:69], .Lst_pace_%=\n"
        "s_add_u32 s50, s49, s5\n"
        "s_add_u32 m0, s77, 2048\n"
        "s_nop 0\n"
        "buffer_load_dwordx4 v0, s[36:39], s50 offen nt lds\n"
        "s_add_u32 m0, s77, 2816\n"
        "s_nop 0\n"
        "buffer_load_dwordx4 v0, s[36:39], s50 offen offset:256 nt lds\n"
        "s_nop 1\n"
        "v_and_b32_dpp v16, v47, v40 row_newbcast:8 row_mask:0xf bank_mask:0xf\n"
        "v_and_b32_dpp v17, v47, v41 row_newbcast:8 row_mask:0xf bank_mask:0xf\n"
        "v_and_b32_dpp v18, v47, v42 row_newbcast:8 row_mask:0xf bank_mask:0xf\n"
        "v_and_b32_dpp v19, v47, v43 row_newbcast:8 row_mask:0xf bank_mask:0xf\n"
        "v_and_b32_dpp v20, v7, v40 row_newbcast:8 row_mask:0xf bank_mask:0xf\n"
        "v_and_b32_dpp v21, v7, v41 row_newbcast:8 row_mask:0xf bank_mask:0xf\n"
        "v_mfma_f32_16x16x32_bf16 v[24:27], v[16:19], v[224:227], v[24:27]\n"
        "v_and_b32_dpp v22, v7, v42 row_newbcast:8 row_mask:0xf bank_mask:0xf\n"
        "v_and_b32_dpp v23, v7, v43 row_newbcast:8 row_mask:0xf bank_mask:0xf\n"
        "v_mfma_f32_16x16x32_bf16 v[28:31], v[16:19], v[228:231], v[28:31]\n"
        "s_nop 1\n"
        "v_mfma_f32_16x16x32_bf16 v[24:27], v[20:23], v[232:235], v[24:27]\n"
        "v_mfma_f32_16x16x32_bf16 v[28:31], v[20:23], v[236:239], v[28:31]\n"
        "s_waitcnt vmcnt(10)\n"
        "s_call_b64 s[68:69], .Lst_pace_%=\n"
        "s_add_u32 s50, s49, s6\n"
        "s_add_u32 m0, s77, 4096\n"
        "s_nop 0\n"
        "buffer_load_dwordx4 v0, s[36:39], s50 offen nt lds\n"
        "s_add_u32 m0, s77, 4864\n"
        "s_nop 0\n"
        "buffer_load_dwordx4 v0, s[36:39], s50 offen offset:256 nt lds\n"
        "v_add_u32 v5, s54, v2\n"
        "ds_read_b128 v[32:35], v3 offset:14336\n"
        "ds_read_b128 v[36:39], v3 offset:15360\n"
        "s_nop 1\n"
        "v_and_b32_dpp v16, v44, v40 row_newbcast:12 row_mask:0xf bank_mask:0xf\n"
        "v_and_b32_dpp v17, v44, v41 row_newbcast:12 row_mask:0xf bank_mask:0xf\n"
        "v_and_b32_dpp v18, v44, v42 row_newbcast:12 row_mask:0xf bank_mask:0xf\n"
        "v_and_b32_dpp v19, v44, v43 row_newbcast:12 row_mask:0xf bank_mask:0xf\n"
        "s_nop 1\n"
        "s_waitcnt lgkmcnt(1)\n"
        "v_mfma_f32_16x16x32_bf16 v[24:27], v[16:19], v[32:35], v[24:27]\n"
        "s_waitcnt lgkmcnt(0)\n"
        "v_mfma_f32_16x16x32_bf16 v[28:31], v[16:19], v[36:39], v[28:31]\n"
        "s_waitcnt vmcnt(10)\n"
        "s_mov_b64 exec, s[86:87]\n"
        "buffer_load_dwordx4 v[8:11], v2, s[44:47], s62 offen offset:0 sc1\n"
        "buffer_load_dwordx4 v[12:15], v2, s[44:47], s62 offen offset:1024 sc1\n"
        "s_mov_b64 exec, -1\n"
        "s_nop 1\n"
        "v_and_b32_dpp v16, v45, v40 row_newbcast:12 row_mask:0xf bank_mask:0xf\n"
        "v_and_b32_dpp v17, v45, v41 row_newbcast:12 row_mask:0xf bank_mask:0xf\n"
        "v_and_b32_dpp v18, v45, v42 row_newbcast:12 row_mask:0xf bank_mask:0xf\n"
        "v_and_b32_dpp v19, v45, v43 row_newbcast:12 row_mask:0xf bank_mask:0xf\n"
        "s_nop 1\n"
        "v_mfma_f32_16x16x32_bf16 v[24:27], v[16:19], v[240:243], v[24:27]\n"
        "v_mfma_f32_16x16x32_bf16 v[28:31], v[16:19], v[244:247], v[28:31]\n"
        "s_waitcnt vmcnt(10)\n"
        "s_nop 1\n"
        "v_and_b32_dpp v16, v46, v40 row_newbcast:12 row_mask:0xf bank_mask:0xf\n"
        "v_and_b32_dpp v17, v46, v41 row_newbcast:12 row_mask:0xf bank_mask:0xf\n"
        "v_and_b32_dpp v18, v46, v42 row_newbcast:12 row_mask:0xf bank_mask:0xf\n"
        "v_and_b32_dpp v19, v46, v43 row_newbcast:12 row_mask:0xf bank_mask:0xf\n"
        "s_nop 1\n"
        "v_mfma_f32_16x16x32_bf16 v[24:27], v[16:19], v[248:251], v[24:27]\n"
        "v_mfma_f32_16x16x32_bf16 v[28:31], v[16:19], v[252:255], v[28:31]\n"
        "s_mul_i32 s64, s51, 143360\n"
        "s_add_u32 s64, s64, s56\n"
        "s_call_b64 s[66:67], .Lst_epi_%=\n"
        "s_waitcnt vmcnt(6)\n"
        "v_max3_u32 v16, v8, v9, v10\n"
        "v_max3_u32 v17, v11, v12, v13\n"
        "v_max3_u32 v16, v16, v14, v15\n"
        "v_max_u32 v16, v16, v17\n"
        "v_cmp_eq_u32 vcc, -1, v16\n"
        "s_cbranch_vccz .Lst_gq2_1_%=\n"
        "s_waitcnt vmcnt(0)\n"
        "v_max3_u32 v16, v8, v9, v10\n"
        "v_max3_u32 v17, v11, v12, v13\n"
        "v_max3_u32 v16, v16, v14, v15\n"
        "v_max_u32 v16, v16, v17\n"
        "v_cmp_eq_u32 vcc, -1, v16\n"
        "s_cbranch_vccz .Lst_gq2_1_%=\n"
        "s_call_b64 s[66:67], .Lst_spinq_%=\n"
        ".Lst_gq2_1_%=:\n"
        "s_mov_b32 s63, s75\n"
        "s_mov_b64 vcc, -1\n"
        "s_call_b64 s[66:67], .Lst_qk0_%=\n"
        "s_call_b64 s[68:69], .Lst_pace_%=\n"
        "s_add_u32 s50, s49, s10\n"
        "buffer_load_dwordx4 a[24:27], v0, s[36:39], s50 offen nt\n"
        "buffer_load_dwordx4 a[28:31], v0, s[36:39], s50 offen offset:256 nt\n"
        "s_barrier\n"
        "s_add_u32 s64, s64, 430080\n"
        "s_call_b64 s[66:67], .Lst_pv0_%=\n"
        "s_add_u32 s51, s51, 1\n"
        "s_mov_b32 s48, s49\n"
        "s_cmp_ge_u32 s51, 3\n"
        "s_cselect_b32 s59, 0, 0x2000000\n"
        "s_add_u32 s49, s49, s59\n"
        "s_mov_b32 s59, s75\n"
        "s_mov_b32 s75, s76\n"
        "s_mov_b32 s76, s77\n"
        "s_mov_b32 s77, s59\n"
        "s_cmp_lt_u32 s51, 4\n"
        "s_cbranch_scc1 .Lst_loop_%=\n"
        "s_add_u32 s62, s55, 229376\n"
        "s_add_u32 s64, s56, 1003520\n"
        "s_mov_b32 s63, s75\n"
        "s_barrier\n"
        "s_call_b64 s[66:67], .Lst_spinq_%=\n"
        "s_mov_b64 vcc, 0\n"
        "s_call_b64 s[66:67], .Lst_qk1_%=\n"
        "s_call_b64 s[66:67], .Lst_pv1_%=\n"
        "s_endpgm\n"
        ".Lst_spinq_%=:\n"
        "s_mov_b64 exec, s[86:87]\n"
        "buffer_load_dwordx4 v[8:11], v2, s[44:47], s62 offen offset:0 sc1\n"
        "buffer_load_dwordx4 v[12:15], v2, s[44:47], s62 offen offset:1024 sc1\n"
        "s_mov_b64 exec, -1\n"
        "s_memrealtime s[72:73]\n"
        "s_waitcnt lgkmcnt(0)\n"
        "s_lshl_b32 s84, s72, 4\n"
        "s_add_u32 s84, s84, 640\n"
        "s_waitcnt vmcnt(0)\n"
        "v_max3_u32 v16, v8, v9, v10\n"
        "v_max3_u32 v17, v11, v12, v13\n"
        "v_max3_u32 v16, v16, v14, v15\n"
        "v_max_u32 v16, v16, v17\n"
        "v_cmp_eq_u32 vcc, -1, v16\n"
        "s_add_u32 s61, s61, 1\n"
        "s_cbranch_vccz .Lst_spinq_done_%=\n"
        "s_cmp_lt_u32 s61, 200000\n"
        "s_cbranch_scc1 .Lst_spinq_%=\n"
        ".Lst_spinq_done_%=:\n"
        "s_setpc_b64 s[66:67]\n"
        ".Lst_pace_%=:\n"
        "s_memrealtime s[72:73]\n"
        "s_waitcnt lgkmcnt(0)\n"
        "s_lshl_b32 s72, s72, 4\n"
        "s_sub_u32 s73, s72, s84\n"
        "s_cmp_lt_i32 s73, 0\n"
        "s_cbranch_scc1 .Lst_pace_%=\n"
        "s_min_i32 s73, s73, s85\n"
        "s_sub_u32 s84, s72, s73\n"
        "s_add_u32 s84, s84, 406\n"
        "s_setpc_b64 s[68:69]\n"
        ".Lst_spinq1_%=:\n"
        "s_mov_b64 exec, s[86:87]\n"
        "buffer_load_dwordx4 v[8:11], v2, s[44:47], s62 offen offset:0 sc1\n"
        "buffer_load_dwordx4 v[12:15], v2, s[44:47], s62 offen offset:1024 sc1\n"
        "buffer_load_dwordx4 v[32:35], v2, s[44:47], s62 offen offset:128 sc1\n"
        "buffer_load_dwordx4 v[36:39], v2, s[44:47], s62 offen offset:1152 sc1\n"
        "s_mov_b64 exec, -1\n"
        "s_waitcnt vmcnt(0)\n"
        ".Lst_chkq1_%=:\n"
        "v_xor_b32 v32, v32, v8\n"
        "v_xor_b32 v32, s78, v32\n"
        "v_xor_b32 v33, v33, v9\n"
        "v_xor_b32 v33, s78, v33\n"
        "v_xor_b32 v34, v34, v10\n"
        "v_xor_b32 v34, s78, v34\n"
        "v_xor_b32 v35, v35, v11\n"
        "v_xor_b32 v35, s78, v35\n"
        "v_xor_b32 v36, v36, v12\n"
        "v_xor_b32 v36, s78, v36\n"
        "v_xor_b32 v37, v37, v13\n"
        "v_xor_b32 v37, s78, v37\n"
        "v_xor_b32 v38, v38, v14\n"
        "v_xor_b32 v38, s78, v38\n"
        "v_xor_b32 v39, v39, v15\n"
        "v_xor_b32 v39, s78, v39\n"
        "v_or3_b32 v32, v32, v33, v34\n"
        "v_or3_b32 v35, v35, v36, v37\n"
        "v_or3_b32 v32, v32, v38, v39\n"
        "v_or_b32 v32, v32, v35\n"
        "v_cmp_ne_u32 vcc, 0, v32\n"
        "s_and_b64 vcc, vcc, s[86:87]\n"
        "s_add_u32 s61, s61, 1\n"
        "s_cbranch_vccz .Lst_spinq_done_%=\n"
        "s_cmp_lt_u32 s61, 200000\n"
        "s_cbranch_scc1 .Lst_spinq1_%=\n"
        "s_setpc_b64 s[66:67]\n"
        ".Lst_qk0_%=:\n"
        "v_add_u32 v6, s63, v2\n"
        "ds_read_b128 v[32:35], v6 offset:0\n"
        "ds_read_b128 v[36:39], v6 offset:1024\n"
        "v_add_u32 v5, s54, v2\n"
        "v_mov_b32 v7, 0xff800000\n"
        "v_mfma_f32_16x16x32_bf16 v[16:19], a[0:3], v[8:11], 0\n"
        "v_mfma_f32_16x16x32_bf16 v[16:19], a[4:7], v[12:15], v[16:19]\n"
        "s_nop 3\n"
        "v_mfma_f32_16x16x32_bf16 v[20:23], a[8:11], v[8:11], 0\n"
        "v_mfma_f32_16x16x32_bf16 v[20:23], a[12:15], v[12:15], v[20:23]\n"
        "s_nop 3\n"
        "v_mfma_f32_16x16x32_bf16 v[24:27], a[16:19], v[8:11], 0\n"
        "v_mov_b32_dpp v44, v16 row_newbcast:0 row_mask:0xf bank_mask:0x1\n"
        "s_nop 0\n"
        "v_mfma_f32_16x16x32_bf16 v[24:27], a[20:23], v[12:15], v[24:27]\n"
        "v_add_f32_dpp v44, v17, v44 row_newbcast:1 row_mask:0xf bank_mask:0x1\n"
        "s_waitcnt lgkmcnt(1)\n"
        "v_mfma_f32_16x16x32_bf16 v[28:31], v[32:35], v[8:11], 0\n"
        "v_mov_b32_dpp v45, v20 row_newbcast:0 row_mask:0xf bank_mask:0x1\n"
        "v_add_f32_dpp v44, v18, v44 row_newbcast:2 row_mask:0xf bank_mask:0x1\n"
        "s_waitcnt lgkmcnt(0)\n"
        "v_mfma_f32_16x16x32_bf16 v[28:31], v[36:39], v[12:15], v[28:31]\n"
        "ds_read_b128 v[32:35], v6 offset:2048\n"
        "ds_read_b128 v[36:39], v6 offset:3072\n"
        "s_cbranch_vccz .Lst_noearly0_3_%=\n"
        "s_add_u32 s84, s84, 406\n"
        "s_add_u32 s50, s49, s7\n"
        "buffer_load_dwordx4 a[0:3], v0, s[36:39], s50 offen nt\n"
        "buffer_load_dwordx4 a[4:7], v0, s[36:39], s50 offen offset:256 nt\n"
        ".Lst_noearly0_3_%=:\n"
        "v_add_f32_dpp v45, v21, v45 row_newbcast:1 row_mask:0xf bank_mask:0x1\n"
        "v_add_f32_dpp v44, v19, v44 row_newbcast:3 row_mask:0xf bank_mask:0x1\n"
        "v_mfma_f32_16x16x32_bf16 v[16:19], a[24:27], v[8:11], 0\n"
        "v_mov_b32_dpp v46, v24 row_newbcast:0 row_mask:0xf bank_mask:0x1\n"
        "v_add_f32_dpp v45, v22, v45 row_newbcast:2 row_mask:0xf bank_mask:0x1\n"
        "v_mfma_f32_16x16x32_bf16 v[16:19], a[28:31], v[12:15], v[16:19]\n"
        "v_add_f32_dpp v46, v25, v46 row_newbcast:1 row_mask:0xf bank_mask:0x1\n"
        "v_add_f32_dpp v45, v23, v45 row_newbcast:3 row_mask:0xf bank_mask:0x1\n"
        "v_mfma_f32_16x16x32_bf16 v[20:23], a[32:35], v[8:11], 0\n"
        "v_mov_b32_dpp v47, v28 row_newbcast:0 row_mask:0xf bank_mask:0x1\n"
        "v_add_f32_dpp v46, v26, v46 row_newbcast:2 row_mask:0xf bank_mask:0x1\n"
        "v_mfma_f32_16x16x32_bf16 v[20:23], a[36:39], v[12:15], v[20:23]\n"
        "v_add_f32_dpp v47, v29, v47 row_newbcast:1 row_mask:0xf bank_mask:0x1\n"
        "v_add_f32_dpp v46, v27, v46 row_newbcast:3 row_mask:0xf bank_mask:0x1\n"
        "v_mfma_f32_16x16x32_bf16 v[24:27], a[40:43], v[8:11], 0\n"
        "v_mov_b32_dpp v7, v16 row_newbcast:0 row_mask:0xf bank_mask:0x1\n"
        "v_add_f32_dpp v47, v30, v47 row_newbcast:2 row_mask:0xf bank_mask:0x1\n"
        "v_mfma_f32_16x16x32_bf16 v[24:27], a[44:47], v[12:15], v[24:27]\n"
        "v_add_f32_dpp v7, v17, v7 row_newbcast:1 row_mask:0xf bank_mask:0x1\n"
        "v_add_f32_dpp v47, v31, v47 row_newbcast:3 row_mask:0xf bank_mask:0x1\n"
        "s_waitcnt lgkmcnt(1)\n"
        "v_mfma_f32_16x16x32_bf16 v[28:31], v[32:35], v[8:11], 0\n"
        "v_mov_b32_dpp v44, v20 row_newbcast:0 row_mask:0xf bank_mask:0x2\n"
        "v_add_f32_dpp v7, v18, v7 row_newbcast:2 row_mask:0xf bank_mask:0x1\n"
        "s_waitcnt lgkmcnt(0)\n"
        "v_mfma_f32_16x16x32_bf16 v[28:31], v[36:39], v[12:15], v[28:31]\n"
        "ds_read_b128 v[32:35], v6 offset:4096\n"
        "ds_read_b128 v[36:39], v6 offset:5120\n"
        "v_add_f32_dpp v44, v21, v44 row_newbcast:1 row_mask:0xf bank_mask:0x2\n"
        "v_add_f32_dpp v7, v19, v7 row_newbcast:3 row_mask:0xf bank_mask:0x1\n"
        "v_mfma_f32_16x16x32_bf16 v[16:19], a[48:51], v[8:11], 0\n"
        "v_mov_b32_dpp v45, v24 row_newbcast:0 row_mask:0xf bank_mask:0x2\n"
        "v_add_f32_dpp v44, v22, v44 row_newbcast:2 row_mask:0xf bank_mask:0x2\n"
        "v_mfma_f32_16x16x32_bf16 v[16:19], a[52:55], v[12:15], v[16:19]\n"
        "s_cbranch_vccz .Lst_noearly0_8_%=\n"
        "s_add_u32 s84, s84, 406\n"
        "s_add_u32 s50, s49, s8\n"
        "buffer_load_dwordx4 a[8:11], v0, s[36:39], s50 offen nt\n"
        "buffer_load_dwordx4 a[12:15], v0, s[36:39], s50 offen offset:256 nt\n"
        ".Lst_noearly0_8_%=:\n"
        "v_add_f32_dpp v45, v25, v45 row_newbcast:1 row_mask:0xf bank_mask:0x2\n"
        "v_add_f32_dpp v44, v23, v44 row_newbcast:3 row_mask:0xf bank_mask:0x2\n"
        "v_mfma_f32_16x16x32_bf16 v[20:23], a[56:59], v[8:11], 0\n"
        "v_mov_b32_dpp v46, v28 row_newbcast:0 row_mask:0xf bank_mask:0x2\n"
        "v_add_f32_dpp v45, v26, v45 row_newbcast:2 row_mask:0xf bank_mask:0x2\n"
        "v_mfma_f32_16x16x32_bf16 v[20:23], a[60:63], v[12:15], v[20:23]\n"
        "v_add_f32_dpp v46, v29, v46 row_newbcast:1 row_mask:0xf bank_mask:0x2\n"
        "v_add_f32_dpp v45, v27, v45 row_newbcast:3 row_mask:0xf bank_mask:0x2\n"
        "v_mfma_f32_16x16x32_bf16 v[24:27], a[64:67], v[8:11], 0\n"
        "v_mov_b32_dpp v47, v16 row_newbcast:0 row_mask:0xf bank_mask:0x2\n"
        "v_add_f32_dpp v46, v30, v46 row_newbcast:2 row_mask:0xf bank_mask:0x2\n"
        "v_mfma_f32_16x16x32_bf16 v[24:27], a[68:71], v[12:15], v[24:27]\n"
        "v_add_f32_dpp v47, v17, v47 row_newbcast:1 row_mask:0xf bank_mask:0x2\n"
        "v_add_f32_dpp v46, v31, v46 row_newbcast:3 row_mask:0xf bank_mask:0x2\n"
        "s_waitcnt lgkmcnt(1)\n"
        "v_mfma_f32_16x16x32_bf16 v[28:31], v[32:35], v[8:11], 0\n"
        "v_mov_b32_dpp v7, v20 row_newbcast:0 row_mask:0xf bank_mask:0x2\n"
        "v_add_f32_dpp v47, v18, v47 row_newbcast:2 row_mask:0xf bank_mask:0x2\n"
        "s_waitcnt lgkmcnt(0)\n"
        "v_mfma_f32_16x16x32_bf16 v[28:31], v[36:39], v[12:15], v[28:31]\n"
        "ds_read_b128 v[32:35], v5 offset:4096\n"
        "ds_read_b128 v[36:39], v5 offset:5120\n"
        "v_add_f32_dpp v7, v21, v7 row_newbcast:1 row_mask:0xf bank_mask:0x2\n"
        "v_add_f32_dpp v47, v19, v47 row_newbcast:3 row_mask:0xf bank_mask:0x2\n"
        "v_mfma_f32_16x16x32_bf16 v[16:19], a[72:75], v[8:11], 0\n"
        "v_mov_b32_dpp v44, v24 row_newbcast:0 row_mask:0xf bank_mask:0x4\n"
        "v_add_f32_dpp v7, v22, v7 row_newbcast:2 row_mask:0xf bank_mask:0x2\n"
        "v_mfma_f32_16x16x32_bf16 v[16:19], a[76:79], v[12:15], v[16:19]\n"
        "v_add_f32_dpp v44, v25, v44 row_newbcast:1 row_mask:0xf bank_mask:0x4\n"
        "v_add_f32_dpp v7, v23, v7 row_newbcast:3 row_mask:0xf bank_mask:0x2\n"
        "v_mfma_f32_16x16x32_bf16 v[20:23], a[80:83], v[8:11], 0\n"
        "v_mov_b32_dpp v45, v28 row_newbcast:0 row_mask:0xf bank_mask:0x4\n"
        "v_add_f32_dpp v44, v26, v44 row_newbcast:2 row_mask:0xf bank_mask:0x4\n"
        "v_mfma_f32_16x16x32_bf16 v[20:23], a[84:87], v[12:15], v[20:23]\n"
        "v_add_f32_dpp v45, v29, v45 row_newbcast:1 row_mask:0xf bank_mask:0x4\n"
        "v_add_f32_dpp v44, v27, v44 row_newbcast:3 row_mask:0xf bank_mask:0x4\n"
        "v_mfma_f32_16x16x32_bf16 v[24:27], a[88:91], v[8:11], 0\n"
        "v_mov_b32_dpp v46, v16 row_newbcast:0 row_mask:0xf bank_mask:0x4\n"
        "v_add_f32_dpp v45, v30, v45 row_newbcast:2 row_mask:0xf bank_mask:0x4\n"
        "v_mfma_f32_16x16x32_bf16 v[24:27], a[92:95], v[12:15], v[24:27]\n"
        "v_add_f32_dpp v46, v17, v46 row_newbcast:1 row_mask:0xf bank_mask:0x4\n"
        "v_add_f32_dpp v45, v31, v45 row_newbcast:3 row_mask:0xf bank_mask:0x4\n"
        "v_mfma_f32_16x16x32_bf16 v[28:31], a[96:99], v[8:11], 0\n"
        "v_mov_b32_dpp v47, v20 row_newbcast:0 row_mask:0xf bank_mask:0x4\n"
        "v_add_f32_dpp v46, v18, v46 row_newbcast:2 row_mask:0xf bank_mask:0x4\n"
        "v_mfma_f32_16x16x32_bf16 v[28:31], a[100:103], v[12:15], v[28:31]\n"
        "s_cbranch_vccz .Lst_noearly0_15_%=\n"
        "s_add_u32 s84, s84, 406\n"
        "s_add_u32 s50, s49, s9\n"
        "buffer_load_dwordx4 a[16:19], v0, s[36:39], s50 offen nt\n"
        "buffer_load_dwordx4 a[20:23], v0, s[36:39], s50 offen offset:256 nt\n"
        ".Lst_noearly0_15_%=:\n"
        "v_add_f32_dpp v47, v21, v47 row_newbcast:1 row_mask:0xf bank_mask:0x4\n"
        "v_add_f32_dpp v46, v19, v46 row_newbcast:3 row_mask:0xf bank_mask:0x4\n"
        "v_mfma_f32_16x16x32_bf16 v[16:19], a[104:107], v[8:11], 0\n"
        "v_mov_b32_dpp v7, v24 row_newbcast:0 row_mask:0xf bank_mask:0x4\n"
        "v_add_f32_dpp v47, v22, v47 row_newbcast:2 row_mask:0xf bank_mask:0x4\n"
        "v_mfma_f32_16x16x32_bf16 v[16:19], a[108:111], v[12:15], v[16:19]\n"
        "v_add_f32_dpp v7, v25, v7 row_newbcast:1 row_mask:0xf bank_mask:0x4\n"
        "v_add_f32_dpp v47, v23, v47 row_newbcast:3 row_mask:0xf bank_mask:0x4\n"
        "v_mfma_f32_16x16x32_bf16 v[20:23], a[112:115], v[8:11], 0\n"
        "v_mov_b32_dpp v44, v28 row_newbcast:0 row_mask:0xf bank_mask:0x8\n"
        "v_add_f32_dpp v7, v26, v7 row_newbcast:2 row_mask:0xf bank_mask:0x4\n"
        "v_mfma_f32_16x16x32_bf16 v[20:23], a[116:119], v[12:15], v[20:23]\n"
        "v_add_f32_dpp v44, v29, v44 row_newbcast:1 row_mask:0xf bank_mask:0x8\n"
        "v_add_f32_dpp v7, v27, v7 row_newbcast:3 row_mask:0xf bank_mask:0x4\n"
        "s_waitcnt lgkmcnt(1)\n"
        "v_mfma_f32_16x16x32_bf16 v[24:27], v[32:35], v[8:11], 0\n"
        "v_mov_b32_dpp v45, v16 row_newbcast:0 row_mask:0xf bank_mask:0x8\n"
        "v_add_f32_dpp v44, v30, v44 row_newbcast:2 row_mask:0xf bank_mask:0x8\n"
        "s_waitcnt lgkmcnt(0)\n"
        "v_mfma_f32_16x16x32_bf16 v[24:27], v[36:39], v[12:15], v[24:27]\n"
        "v_add_f32_dpp v45, v17, v45 row_newbcast:1 row_mask:0xf bank_mask:0x8\n"
        "v_add_f32_dpp v44, v31, v44 row_newbcast:3 row_mask:0xf bank_mask:0x8\n"
        "s_nop 0\n"
        "v_mov_b32_dpp v46, v20 row_newbcast:0 row_mask:0xf bank_mask:0x8\n"
        "v_add_f32_dpp v45, v18, v45 row_newbcast:2 row_mask:0xf bank_mask:0x8\n"
        "s_nop 0\n"
        "v_add_f32_dpp v46, v21, v46 row_newbcast:1 row_mask:0xf bank_mask:0x8\n"
        "v_add_f32_dpp v45, v19, v45 row_newbcast:3 row_mask:0xf bank_mask:0x8\n"
        "s_nop 0\n"
        "v_mov_b32_dpp v47, v24 row_newbcast:0 row_mask:0xf bank_mask:0x8\n"
        "v_add_f32_dpp v46, v22, v46 row_newbcast:2 row_mask:0xf bank_mask:0x8\n"
        "s_nop 0\n"
        "v_add_f32_dpp v47, v25, v47 row_newbcast:1 row_mask:0xf bank_mask:0x8\n"
        "v_add_f32_dpp v46, v23, v46 row_newbcast:3 row_mask:0xf bank_mask:0x8\n"
        "s_nop 0\n"
        "s_nop 0\n"
        "v_add_f32_dpp v47, v26, v47 row_newbcast:2 row_mask:0xf bank_mask:0x8\n"
        "s_nop 0\n"
        "s_nop 0\n"
        "v_add_f32_dpp v47, v27, v47 row_newbcast:3 row_mask:0xf bank_mask:0x8\n"
        "s_branch .Lst_softmax_%=\n"
        ".Lst_qk1_%=:\n"
        "v_add_u32 v6, s63, v2\n"
        "ds_read_b128 v[32:35], v6 offset:0\n"
        "ds_read_b128 v[36:39], v6 offset:1024\n"
        "v_add_u32 v5, s54, v2\n"
        "v_mov_b32 v7, 0xff800000\n"
        "v_mfma_f32_16x16x32_bf16 v[16:19], a[232:235], v[8:11], 0\n"
        "v_mfma_f32_16x16x32_bf16 v[16:19], a[236:239], v[12:15], v[16:19]\n"
        "s_nop 3\n"
        "v_mfma_f32_16x16x32_bf16 v[20:23], a[240:243], v[8:11], 0\n"
        "v_mfma_f32_16x16x32_bf16 v[20:23], a[244:247], v[12:15], v[20:23]\n"
        "s_nop 3\n"
        "v_mfma_f32_16x16x32_bf16 v[24:27], a[248:251], v[8:11], 0\n"
        "v_mov_b32_dpp v44, v16 row_newbcast:0 row_mask:0xf bank_mask:0x1\n"
        "s_nop 0\n"
        "v_mfma_f32_16x16x32_bf16 v[24:27], a[252:255], v[12:15], v[24:27]\n"
        "v_add_f32_dpp v44, v17, v44 row_newbcast:1 row_mask:0xf bank_mask:0x1\n"
        "s_waitcnt lgkmcnt(1)\n"
        "v_mfma_f32_16x16x32_bf16 v[28:31], v[32:35], v[8:11], 0\n"
        "v_mov_b32_dpp v45, v20 row_newbcast:0 row_mask:0xf bank_mask:0x1\n"
        "v_add_f32_dpp v44, v18, v44 row_newbcast:2 row_mask:0xf bank_mask:0x1\n"
        "s_waitcnt lgkmcnt(0)\n"
        "v_mfma_f32_16x16x32_bf16 v[28:31], v[36:39], v[12:15], v[28:31]\n"
        "ds_read_b128 v[32:35], v6 offset:2048\n"
        "ds_read_b128 v[36:39], v6 offset:3072\n"
        "s_cbranch_vccz .Lst_noearly1_3_%=\n"
        "s_add_u32 s84, s84, 406\n"
        "s_add_u32 s50, s49, s7\n"
        "buffer_load_dwordx4 a[232:235], v0, s[36:39], s50 offen nt\n"
        "buffer_load_dwordx4 a[236:239], v0, s[36:39], s50 offen offset:256 nt\n"
        ".Lst_noearly1_3_%=:\n"
        "v_add_f32_dpp v45, v21, v45 row_newbcast:1 row_mask:0xf bank_mask:0x1\n"
        "v_add_f32_dpp v44, v19, v44 row_newbcast:3 row_mask:0xf bank_mask:0x1\n"
        "v_mfma_f32_16x16x32_bf16 v[16:19], v[48:51], v[8:11], 0\n"
        "v_mov_b32_dpp v46, v24 row_newbcast:0 row_mask:0xf bank_mask:0x1\n"
        "v_add_f32_dpp v45, v22, v45 row_newbcast:2 row_mask:0xf bank_mask:0x1\n"
        "v_mfma_f32_16x16x32_bf16 v[16:19], v[52:55], v[12:15], v[16:19]\n"
        "v_add_f32_dpp v46, v25, v46 row_newbcast:1 row_mask:0xf bank_mask:0x1\n"
        "v_add_f32_dpp v45, v23, v45 row_newbcast:3 row_mask:0xf bank_mask:0x1\n"
        "v_mfma_f32_16x16x32_bf16 v[20:23], v[56:59], v[8:11], 0\n"
        "v_mov_b32_dpp v47, v28 row_newbcast:0 row_mask:0xf bank_mask:0x1\n"
        "v_add_f32_dpp v46, v26, v46 row_newbcast:2 row_mask:0xf bank_mask:0x1\n"
        "v_mfma_f32_16x16x32_bf16 v[20:23], v[60:63], v[12:15], v[20:23]\n"
        "v_add_f32_dpp v47, v29, v47 row_newbcast:1 row_mask:0xf bank_mask:0x1\n"
        "v_add_f32_dpp v46, v27, v46 row_newbcast:3 row_mask:0xf bank_mask:0x1\n"
        "v_mfma_f32_16x16x32_bf16 v[24:27], v[64:67], v[8:11], 0\n"
        "v_mov_b32_dpp v7, v16 row_newbcast:0 row_mask:0xf bank_mask:0x1\n"
        "v_add_f32_dpp v47, v30, v47 row_newbcast:2 row_mask:0xf bank_mask:0x1\n"
        "v_mfma_f32_16x16x32_bf16 v[24:27], v[68:71], v[12:15], v[24:27]\n"
        "v_add_f32_dpp v7, v17, v7 row_newbcast:1 row_mask:0xf bank_mask:0x1\n"
        "v_add_f32_dpp v47, v31, v47 row_newbcast:3 row_mask:0xf bank_mask:0x1\n"
        "s_waitcnt lgkmcnt(1)\n"
        "v_mfma_f32_16x16x32_bf16 v[28:31], v[32:35], v[8:11], 0\n"
        "v_mov_b32_dpp v44, v20 row_newbcast:0 row_mask:0xf bank_mask:0x2\n"
        "v_add_f32_dpp v7, v18, v7 row_newbcast:2 row_mask:0xf bank_mask:0x1\n"
        "s_waitcnt lgkmcnt(0)\n"
        "v_mfma_f32_16x16x32_bf16 v[28:31], v[36:39], v[12:15], v[28:31]\n"
        "ds_read_b128 v[32:35], v6 offset:4096\n"
        "ds_read_b128 v[36:39], v6 offset:5120\n"
        "v_add_f32_dpp v44, v21, v44 row_newbcast:1 row_mask:0xf bank_mask:0x2\n"
        "v_add_f32_dpp v7, v19, v7 row_newbcast:3 row_mask:0xf bank_mask:0x1\n"
        "v_mfma_f32_16x16x32_bf16 v[16:19], v[72:75], v[8:11], 0\n"
        "v_mov_b32_dpp v45, v24 row_newbcast:0 row_mask:0xf bank_mask:0x2\n"
        "v_add_f32_dpp v44, v22, v44 row_newbcast:2 row_mask:0xf bank_mask:0x2\n"
        "v_mfma_f32_16x16x32_bf16 v[16:19], v[76:79], v[12:15], v[16:19]\n"
        "s_cbranch_vccz .Lst_noearly1_8_%=\n"
        "s_add_u32 s84, s84, 406\n"
        "s_add_u32 s50, s49, s8\n"
        "buffer_load_dwordx4 a[240:243], v0, s[36:39], s50 offen nt\n"
        "buffer_load_dwordx4 a[244:247], v0, s[36:39], s50 offen offset:256 nt\n"
        ".Lst_noearly1_8_%=:\n"
        "v_add_f32_dpp v45, v25, v45 row_newbcast:1 row_mask:0xf bank_mask:0x2\n"
        "v_add_f32_dpp v44, v23, v44 row_newbcast:3 row_mask:0xf bank_mask:0x2\n"
        "v_mfma_f32_16x16x32_bf16 v[20:23], v[80:83], v[8:11], 0\n"
        "v_mov_b32_dpp v46, v28 row_newbcast:0 row_mask:0xf bank_mask:0x2\n"
        "v_add_f32_dpp v45, v26, v45 row_newbcast:2 row_mask:0xf bank_mask:0x2\n"
        "v_mfma_f32_16x16x32_bf16 v[20:23], v[84:87], v[12:15], v[20:23]\n"
        "v_add_f32_dpp v46, v29, v46 row_newbcast:1 row_mask:0xf bank_mask:0x2\n"
        "v_add_f32_dpp v45, v27, v45 row_newbcast:3 row_mask:0xf bank_mask:0x2\n"
        "v_mfma_f32_16x16x32_bf16 v[24:27], v[88:91], v[8:11], 0\n"
        "v_mov_b32_dpp v47, v16 row_newbcast:0 row_mask:0xf bank_mask:0x2\n"
        "v_add_f32_dpp v46, v30, v46 row_newbcast:2 row_mask:0xf bank_mask:0x2\n"
        "v_mfma_f32_16x16x32_bf16 v[24:27], v[92:95], v[12:15], v[24:27]\n"
        "v_add_f32_dpp v47, v17, v47 row_newbcast:1 row_mask:0xf bank_mask:0x2\n"
        "v_add_f32_dpp v46, v31, v46 row_newbcast:3 row_mask:0xf bank_mask:0x2\n"
        "s_waitcnt lgkmcnt(1)\n"
        "v_mfma_f32_16x16x32_bf16 v[28:31], v[32:35], v[8:11], 0\n"
        "v_mov_b32_dpp v7, v20 row_newbcast:0 row_mask:0xf bank_mask:0x2\n"
        "v_add_f32_dpp v47, v18, v47 row_newbcast:2 row_mask:0xf bank_mask:0x2\n"
        "s_waitcnt lgkmcnt(0)\n"
        "v_mfma_f32_16x16x32_bf16 v[28:31], v[36:39], v[12:15], v[28:31]\n"
        "ds_read_b128 v[32:35], v5 offset:12288\n"
        "ds_read_b128 v[36:39], v5 offset:13312\n"
        "v_add_f32_dpp v7, v21, v7 row_newbcast:1 row_mask:0xf bank_mask:0x2\n"
        "v_add_f32_dpp v47, v19, v47 row_newbcast:3 row_mask:0xf bank_mask:0x2\n"
        "v_mfma_f32_16x16x32_bf16 v[16:19], v[96:99], v[8:11], 0\n"
        "v_mov_b32_dpp v44, v24 row_newbcast:0 row_mask:0xf bank_mask:0x4\n"
        "v_add_f32_dpp v7, v22, v7 row_newbcast:2 row_mask:0xf bank_mask:0x2\n"
        "v_mfma_f32_16x16x32_bf16 v[16:19], v[100:103], v[12:15], v[16:19]\n"
        "v_add_f32_dpp v44, v25, v44 row_newbcast:1 row_mask:0xf bank_mask:0x4\n"
        "v_add_f32_dpp v7, v23, v7 row_newbcast:3 row_mask:0xf bank_mask:0x2\n"
        "v_mfma_f32_16x16x32_bf16 v[20:23], v[104:107], v[8:11], 0\n"
        "v_mov_b32_dpp v45, v28 row_newbcast:0 row_mask:0xf bank_mask:0x4\n"
        "v_add_f32_dpp v44, v26, v44 row_newbcast:2 row_mask:0xf bank_mask:0x4\n"
        "v_mfma_f32_16x16x32_bf16 v[20:23], v[108:111], v[12:15], v[20:23]\n"
        "v_add_f32_dpp v45, v29, v45 row_newbcast:1 row_mask:0xf bank_mask:0x4\n"
        "v_add_f32_dpp v44, v27, v44 row_newbcast:3 row_mask:0xf bank_mask:0x4\n"
        "v_mfma_f32_16x16x32_bf16 v[24:27], v[112:115], v[8:11], 0\n"
        "v_mov_b32_dpp v46, v16 row_newbcast:0 row_mask:0xf bank_mask:0x4\n"
        "v_add_f32_dpp v45, v30, v45 row_newbcast:2 row_mask:0xf bank_mask:0x4\n"
        "v_mfma_f32_16x16x32_bf16 v[24:27], v[116:119], v[12:15], v[24:27]\n"
        "v_add_f32_dpp v46, v17, v46 row_newbcast:1 row_mask:0xf bank_mask:0x4\n"
        "v_add_f32_dpp v45, v31, v45 row_newbcast:3 row_mask:0xf bank_mask:0x4\n"
        "v_mfma_f32_16x16x32_bf16 v[28:31], v[120:123], v[8:11], 0\n"
        "v_mov_b32_dpp v47, v20 row_newbcast:0 row_mask:0xf bank_mask:0x4\n"
        "v_add_f32_dpp v46, v18, v46 row_newbcast:2 row_mask:0xf bank_mask:0x4\n"
        "v_mfma_f32_16x16x32_bf16 v[28:31], v[124:127], v[12:15], v[28:31]\n"
        "s_cbranch_vccz .Lst_noearly1_15_%=\n"
        "s_add_u32 s84, s84, 406\n"
        "s_add_u32 s50, s49, s9\n"
        "buffer_load_dwordx4 a[248:251], v0, s[36:39], s50 offen nt\n"
        "buffer_load_dwordx4 a[252:255], v0, s[36:39], s50 offen offset:256 nt\n"
        ".Lst_noearly1_15_%=:\n"
        "v_add_f32_dpp v47, v21, v47 row_newbcast:1 row_mask:0xf bank_mask:0x4\n"
        "v_add_f32_dpp v46, v19, v46 row_newbcast:3 row_mask:0xf bank_mask:0x4\n"
        "v_mfma_f32_16x16x32_bf16 v[16:19], v[128:131], v[8:11], 0\n"
        "v_mov_b32_dpp v7, v24 row_newbcast:0 row_mask:0xf bank_mask:0x4\n"
        "v_add_f32_dpp v47, v22, v47 row_newbcast:2 row_mask:0xf bank_mask:0x4\n"
        "v_mfma_f32_16x16x32_bf16 v[16:19], v[132:135], v[12:15], v[16:19]\n"
        "v_add_f32_dpp v7, v25, v7 row_newbcast:1 row_mask:0xf bank_mask:0x4\n"
        "v_add_f32_dpp v47, v23, v47 row_newbcast:3 row_mask:0xf bank_mask:0x4\n"
        "v_mfma_f32_16x16x32_bf16 v[20:23], v[136:139], v[8:11], 0\n"
        "v_mov_b32_dpp v44, v28 row_newbcast:0 row_mask:0xf bank_mask:0x8\n"
        "v_add_f32_dpp v7, v26, v7 row_newbcast:2 row_mask:0xf bank_mask:0x4\n"
        "v_mfma_f32_16x16x32_bf16 v[20:23], v[140:143], v[12:15], v[20:23]\n"
        "v_add_f32_dpp v44, v29, v44 row_newbcast:1 row_mask:0xf bank_mask:0x8\n"
        "v_add_f32_dpp v7, v27, v7 row_newbcast:3 row_mask:0xf bank_mask:0x4\n"
        "s_waitcnt lgkmcnt(1)\n"
        "v_mfma_f32_16x16x32_bf16 v[24:27], v[32:35], v[8:11], 0\n"
        "v_mov_b32_dpp v45, v16 row_newbcast:0 row_mask:0xf bank_mask:0x8\n"
        "v_add_f32_dpp v44, v30, v44 row_newbcast:2 row_mask:0xf bank_mask:0x8\n"
        "s_waitcnt lgkmcnt(0)\n"
        "v_mfma_f32_16x16x32_bf16 v[24:27], v[36:39], v[12:15], v[24:27]\n"
        "v_add_f32_dpp v45, v17, v45 row_newbcast:1 row_mask:0xf bank_mask:0x8\n"
        "v_add_f32_dpp v44, v31, v44 row_newbcast:3 row_mask:0xf bank_mask:0x8\n"
        "s_nop 0\n"
        "v_mov_b32_dpp v46, v20 row_newbcast:0 row_mask:0xf bank_mask:0x8\n"
        "v_add_f32_dpp v45, v18, v45 row_newbcast:2 row_mask:0xf bank_mask:0x8\n"
        "s_nop 0\n"
        "v_add_f32_dpp v46, v21, v46 row_newbcast:1 row_mask:0xf bank_mask:0x8\n"
        "v_add_f32_dpp v45, v19, v45 row_newbcast:3 row_mask:0xf bank_mask:0x8\n"
        "s_nop 0\n"
        "v_mov_b32_dpp v47, v24 row_newbcast:0 row_mask:0xf bank_mask:0x8\n"
        "v_add_f32_dpp v46, v22, v46 row_newbcast:2 row_mask:0xf bank_mask:0x8\n"
        "s_nop 0\n"
        "v_add_f32_dpp v47, v25, v47 row_newbcast:1 row_mask:0xf bank_mask:0x8\n"
        "v_add_f32_dpp v46, v23, v46 row_newbcast:3 row_mask:0xf bank_mask:0x8\n"
        "s_nop 0\n"
        "s_nop 0\n"
        "v_add_f32_dpp v47, v26, v47 row_newbcast:2 row_mask:0xf bank_mask:0x8\n"
        "s_nop 0\n"
        "s_nop 0\n"
        "v_add_f32_dpp v47, v27, v47 row_newbcast:3 row_mask:0xf bank_mask:0x8\n"
        ".Lst_softmax_%=:\n"
        "s_cmp_lg_u32 s52, 0\n"
        "s_cbranch_scc1 .Lst_sm19_%=\n"
        "v_mov_b32 v16, 0xff800000\n"
        "s_nop 1\n"
        "v_mov_b32_dpp v47, v16 quad_perm:[0,1,2,3] row_mask:0xf bank_mask:0x8\n"
        ".Lst_sm19_%=:\n"
        "v_max3_f32 v16, v44, v45, v46\n"
        "v_max3_f32 v16, v16, v47, v7\n"
        "s_nop 1\n"
        "v_max_f32_dpp v16, v16, v16 row_ror:4 row_mask:0xf bank_mask:0xf\n"
        "s_nop 1\n"
        "v_max_f32_dpp v16, v16, v16 row_ror:8 row_mask:0xf bank_mask:0xf\n"
        "s_nop 1\n"
        "v_max_f32_dpp v16, v16, v16 row_bcast:15 row_mask:0xa bank_mask:0xf\n"
        "s_nop 1\n"
        "v_max_f32_dpp v16, v16, v16 row_bcast:31 row_mask:0xc bank_mask:0xf\n"
        "v_mul_f32 v16, s57, v16\n"
        "v_mov_b32 v17, s57\n"
        "v_readlane_b32 s58, v16, 63\n"
        "s_nop 1\n"
        "v_fma_f32 v44, v44, v17, -s58\n"
        "v_fma_f32 v45, v45, v17, -s58\n"
        "v_fma_f32 v46, v46, v17, -s58\n"
        "v_fma_f32 v47, v47, v17, -s58\n"
        "v_fma_f32 v7, v7, v17, -s58\n"
        "v_exp_f32 v44, v44\n"
        "v_exp_f32 v45, v45\n"
        "v_exp_f32 v46, v46\n"
        "v_exp_f32 v47, v47\n"
        "v_exp_f32 v7, v7\n"
        "s_nop 0\n"
        "v_cvt_pk_bf16_f32 v44, v44, v44\n"
        "v_cvt_pk_bf16_f32 v45, v45, v45\n"
        "v_cvt_pk_bf16_f32 v46, v46, v46\n"
        "v_cvt_pk_bf16_f32 v47, v47, v47\n"
        "v_cvt_pk_bf16_f32 v7, v7, v7\n"
        "s_setpc_b64 s[66:67]\n"
        ".Lst_pv0_%=:\n"
        "v_add_u32 v5, s54, v2\n"
        "ds_read_b128 v[32:35], v3 offset:0\n"
        "ds_read_b128 v[36:39], v3 offset:1024\n"
        "s_nop 1\n"
        "v_and_b32_dpp v16, v44, v40 row_newbcast:0 row_mask:0xf bank_mask:0xf\n"
        "v_and_b32_dpp v17, v44, v41 row_newbcast:0 row_mask:0xf bank_mask:0xf\n"
        "v_and_b32_dpp v18, v44, v42 row_newbcast:0 row_mask:0xf bank_mask:0xf\n"
        "v_and_b32_dpp v19, v44, v43 row_newbcast:0 row_mask:0xf bank_mask:0xf\n"
        "v_and_b32_dpp v20, v45, v40 row_newbcast:0 row_mask:0xf bank_mask:0xf\n"
        "v_and_b32_dpp v21, v45, v41 row_newbcast:0 row_mask:0xf bank_mask:0xf\n"
        "v_mfma_f32_16x16x32_bf16 v[24:27], v[16:19], a[120:123], 0\n"
        "v_and_b32_dpp v22, v45, v42 row_newbcast:0 row_mask:0xf bank_mask:0xf\n"
        "v_and_b32_dpp v23, v45, v43 row_newbcast:0 row_mask:0xf bank_mask:0xf\n"
        "v_mfma_f32_16x16x32_bf16 v[28:31], v[16:19], a[124:127], 0\n"
        "v_and_b32_dpp v16, v46, v40 row_newbcast:0 row_mask:0xf bank_mask:0xf\n"
        "v_and_b32_dpp v17, v46, v41 row_newbcast:0 row_mask:0xf bank_mask:0xf\n"
        "v_mfma_f32_16x16x32_bf16 v[24:27], v[20:23], a[128:131], v[24:27]\n"
        "v_and_b32_dpp v18, v46, v42 row_newbcast:0 row_mask:0xf bank_mask:0xf\n"
        "v_and_b32_dpp v19, v46, v43 row_newbcast:0 row_mask:0xf bank_mask:0xf\n"
        "v_mfma_f32_16x16x32_bf16 v[28:31], v[20:23], a[132:135], v[28:31]\n"
        "v_and_b32_dpp v20, v47, v40 row_newbcast:0 row_mask:0xf bank_mask:0xf\n"
        "v_and_b32_dpp v21, v47, v41 row_newbcast:0 row_mask:0xf bank_mask:0xf\n"
        "v_mfma_f32_16x16x32_bf16 v[24:27], v[16:19], a[136:139], v[24:27]\n"
        "v_and_b32_dpp v22, v47, v42 row_newbcast:0 row_mask:0xf bank_mask:0xf\n"
        "v_and_b32_dpp v23, v47, v43 row_newbcast:0 row_mask:0xf bank_mask:0xf\n"
        "v_mfma_f32_16x16x32_bf16 v[28:31], v[16:19], a[140:143], v[28:31]\n"
        "v_and_b32_dpp v16, v7, v40 row_newbcast:0 row_mask:0xf bank_mask:0xf\n"
        "v_and_b32_dpp v17, v7, v41 row_newbcast:0 row_mask:0xf bank_mask:0xf\n"
        "s_waitcnt lgkmcnt(1)\n"
        "v_mfma_f32_16x16x32_bf16 v[24:27], v[20:23], v[32:35], v[24:27]\n"
        "v_and_b32_dpp v18, v7, v42 row_newbcast:0 row_mask:0xf bank_mask:0xf\n"
        "v_and_b32_dpp v19, v7, v43 row_newbcast:0 row_mask:0xf bank_mask:0xf\n"
        "s_waitcnt lgkmcnt(0)\n"
        "v_mfma_f32_16x16x32_bf16 v[28:31], v[20:23], v[36:39], v[28:31]\n"
        "ds_read_b128 v[32:35], v3 offset:2048\n"
        "ds_read_b128 v[36:39], v3 offset:3072\n"
        "v_and_b32_dpp v20, v44, v40 row_newbcast:4 row_mask:0xf bank_mask:0xf\n"
        "v_and_b32_dpp v21, v44, v41 row_newbcast:4 row_mask:0xf bank_mask:0xf\n"
        "v_mfma_f32_16x16x32_bf16 v[24:27], v[16:19], a[144:147], v[24:27]\n"
        "v_and_b32_dpp v22, v44, v42 row_newbcast:4 row_mask:0xf bank_mask:0xf\n"
        "v_and_b32_dpp v23, v44, v43 row_newbcast:4 row_mask:0xf bank_mask:0xf\n"
        "v_mfma_f32_16x16x32_bf16 v[28:31], v[16:19], a[148:151], v[28:31]\n"
        "v_and_b32_dpp v16, v45, v40 row_newbcast:4 row_mask:0xf bank_mask:0xf\n"
        "v_and_b32_dpp v17, v45, v41 row_newbcast:4 row_mask:0xf bank_mask:0xf\n"
        "v_mfma_f32_16x16x32_bf16 v[24:27], v[20:23], a[152:155], v[24:27]\n"
        "v_and_b32_dpp v18, v45, v42 row_newbcast:4 row_mask:0xf bank_mask:0xf\n"
        "v_and_b32_dpp v19, v45, v43 row_newbcast:4 row_mask:0xf bank_mask:0xf\n"
        "v_mfma_f32_16x16x32_bf16 v[28:31], v[20:23], a[156:159], v[28:31]\n"
        "v_and_b32_dpp v20, v46, v40 row_newbcast:4 row_mask:0xf bank_mask:0xf\n"
        "v_and_b32_dpp v21, v46, v41 row_newbcast:4 row_mask:0xf bank_mask:0xf\n"
        "v_mfma_f32_16x16x32_bf16 v[24:27], v[16:19], a[160:163], v[24:27]\n"
        "v_and_b32_dpp v22, v46, v42 row_newbcast:4 row_mask:0xf bank_mask:0xf\n"
        "v_and_b32_dpp v23, v46, v43 row_newbcast:4 row_mask:0xf bank_mask:0xf\n"
        "v_mfma_f32_16x16x32_bf16 v[28:31], v[16:19], a[164:167], v[28:31]\n"
        "v_and_b32_dpp v16, v47, v40 row_newbcast:4 row_mask:0xf bank_mask:0xf\n"
        "v_and_b32_dpp v17, v47, v41 row_newbcast:4 row_mask:0xf bank_mask:0xf\n"
        "s_waitcnt lgkmcnt(1)\n"
        "v_mfma_f32_16x16x32_bf16 v[24:27], v[20:23], v[32:35], v[24:27]\n"
        "v_and_b32_dpp v18, v47, v42 row_newbcast:4 row_mask:0xf bank_mask:0xf\n"
        "v_and_b32_dpp v19, v47, v43 row_newbcast:4 row_mask:0xf bank_mask:0xf\n"
        "s_waitcnt lgkmcnt(0)\n"
        "v_mfma_f32_16x16x32_bf16 v[28:31], v[20:23], v[36:39], v[28:31]\n"
        "ds_read_b128 v[32:35], v3 offset:4096\n"
        "ds_read_b128 v[36:39], v3 offset:5120\n"
        "v_and_b32_dpp v20, v7, v40 row_newbcast:4 row_mask:0xf bank_mask:0xf\n"
        "v_and_b32_dpp v21, v7, v41 row_newbcast:4 row_mask:0xf bank_mask:0xf\n"
        "v_mfma_f32_16x16x32_bf16 v[24:27], v[16:19], a[168:171], v[24:27]\n"
        "v_and_b32_dpp v22, v7, v42 row_newbcast:4 row_mask:0xf bank_mask:0xf\n"
        "v_and_b32_dpp v23, v7, v43 row_newbcast:4 row_mask:0xf bank_mask:0xf\n"
        "v_mfma_f32_16x16x32_bf16 v[28:31], v[16:19], a[172:175], v[28:31]\n"
        "s_add_u32 s84, s84, 406\n"
        "s_add_u32 s50, s49, s11\n"
        "buffer_load_dwordx4 a[32:35], v0, s[36:39], s50 offen nt\n"
        "buffer_load_dwordx4 a[36:39], v0, s[36:39], s50 offen offset:256 nt\n"
        "v_and_b32_dpp v16, v44, v40 row_newbcast:8 row_mask:0xf bank_mask:0xf\n"
        "v_and_b32_dpp v17, v44, v41 row_newbcast:8 row_mask:0xf bank_mask:0xf\n"
        "v_mfma_f32_16x16x32_bf16 v[24:27], v[20:23], a[176:179], v[24:27]\n"
        "v_and_b32_dpp v18, v44, v42 row_newbcast:8 row_mask:0xf bank_mask:0xf\n"
        "v_and_b32_dpp v19, v44, v43 row_newbcast:8 row_mask:0xf bank_mask:0xf\n"
        "v_mfma_f32_16x16x32_bf16 v[28:31], v[20:23], a[180:183], v[28:31]\n"
        "v_and_b32_dpp v20, v45, v40 row_newbcast:8 row_mask:0xf bank_mask:0xf\n"
        "v_and_b32_dpp v21, v45, v41 row_newbcast:8 row_mask:0xf bank_mask:0xf\n"
        "v_mfma_f32_16x16x32_bf16 v[24:27], v[16:19], a[184:187], v[24:27]\n"
        "v_and_b32_dpp v22, v45, v42 row_newbcast:8 row_mask:0xf bank_mask:0xf\n"
        "v_and_b32_dpp v23, v45, v43 row_newbcast:8 row_mask:0xf bank_mask:0xf\n"
        "v_mfma_f32_16x16x32_bf16 v[28:31], v[16:19], a[188:191], v[28:31]\n"
        "v_and_b32_dpp v16, v46, v40 row_newbcast:8 row_mask:0xf bank_mask:0xf\n"
        "v_and_b32_dpp v17, v46, v41 row_newbcast:8 row_mask:0xf bank_mask:0xf\n"
        "s_waitcnt lgkmcnt(1)\n"
        "v_mfma_f32_16x16x32_bf16 v[24:27], v[20:23], v[32:35], v[24:27]\n"
        "v_and_b32_dpp v18, v46, v42 row_newbcast:8 row_mask:0xf bank_mask:0xf\n"
        "v_and_b32_dpp v19, v46, v43 row_newbcast:8 row_mask:0xf bank_mask:0xf\n"
        "s_waitcnt lgkmcnt(0)\n"
        "v_mfma_f32_16x16x32_bf16 v[28:31], v[20:23], v[36:39], v[28:31]\n"
        "ds_read_b128 v[32:35], v3 offset:6144\n"
        "ds_read_b128 v[36:39], v3 offset:7168\n"
        "v_and_b32_dpp v20, v47, v40 row_newbcast:8 row_mask:0xf bank_mask:0xf\n"
        "v_and_b32_dpp v21, v47, v41 row_newbcast:8 row_mask:0xf bank_mask:0xf\n"
        "v_mfma_f32_16x16x32_bf16 v[24:27], v[16:19], a[192:195], v[24:27]\n"
        "v_and_b32_dpp v22, v47, v42 row_newbcast:8 row_mask:0xf bank_mask:0xf\n"
        "v_and_b32_dpp v23, v47, v43 row_newbcast:8 row_mask:0xf bank_mask:0xf\n"
        "v_mfma_f32_16x16x32_bf16 v[28:31], v[16:19], a[196:199], v[28:31]\n"
        "v_and_b32_dpp v16, v7, v40 row_newbcast:8 row_mask:0xf bank_mask:0xf\n"
        "v_and_b32_dpp v17, v7, v41 row_newbcast:8 row_mask:0xf bank_mask:0xf\n"
        "v_mfma_f32_16x16x32_bf16 v[24:27], v[20:23], a[200:203], v[24:27]\n"
        "v_and_b32_dpp v18, v7, v42 row_newbcast:8 row_mask:0xf bank_mask:0xf\n"
        "v_and_b32_dpp v19, v7, v43 row_newbcast:8 row_mask:0xf bank_mask:0xf\n"
        "v_mfma_f32_16x16x32_bf16 v[28:31], v[20:23], a[204:207], v[28:31]\n"
        "v_and_b32_dpp v20, v44, v40 row_newbcast:12 row_mask:0xf bank_mask:0xf\n"
        "v_and_b32_dpp v21, v44, v41 row_newbcast:12 row_mask:0xf bank_mask:0xf\n"
        "v_mfma_f32_16x16x32_bf16 v[24:27], v[16:19], a[208:211], v[24:27]\n"
        "v_and_b32_dpp v22, v44, v42 row_newbcast:12 row_mask:0xf bank_mask:0xf\n"
        "v_and_b32_dpp v23, v44, v43 row_newbcast:12 row_mask:0xf bank_mask:0xf\n"
        "v_mfma_f32_16x16x32_bf16 v[28:31], v[16:19], a[212:215], v[28:31]\n"
        "v_and_b32_dpp v16, v45, v40 row_newbcast:12 row_mask:0xf bank_mask:0xf\n"
        "v_and_b32_dpp v17, v45, v41 row_newbcast:12 row_mask:0xf bank_mask:0xf\n"
        "s_waitcnt lgkmcnt(1)\n"
        "v_mfma_f32_16x16x32_bf16 v[24:27], v[20:23], v[32:35], v[24:27]\n"
        "v_and_b32_dpp v18, v45, v42 row_newbcast:12 row_mask:0xf bank_mask:0xf\n"
        "v_and_b32_dpp v19, v45, v43 row_newbcast:12 row_mask:0xf bank_mask:0xf\n"
        "s_waitcnt lgkmcnt(0)\n"
        "v_mfma_f32_16x16x32_bf16 v[28:31], v[20:23], v[36:39], v[28:31]\n"
        "ds_read_b128 v[32:35], v5 offset:0\n"
        "ds_read_b128 v[36:39], v5 offset:1024\n"
        "v_and_b32_dpp v20, v46, v40 row_newbcast:12 row_mask:0xf bank_mask:0xf\n"
        "v_and_b32_dpp v21, v46, v41 row_newbcast:12 row_mask:0xf bank_mask:0xf\n"
        "v_mfma_f32_16x16x32_bf16 v[24:27], v[16:19], a[216:219], v[24:27]\n"
        "v_and_b32_dpp v22, v46, v42 row_newbcast:12 row_mask:0xf bank_mask:0xf\n"
        "v_and_b32_dpp v23, v46, v43 row_newbcast:12 row_mask:0xf bank_mask:0xf\n"
        "v_mfma_f32_16x16x32_bf16 v[28:31], v[16:19], a[220:223], v[28:31]\n"
        "v_and_b32_dpp v16, v47, v40 row_newbcast:12 row_mask:0xf bank_mask:0xf\n"
        "v_and_b32_dpp v17, v47, v41 row_newbcast:12 row_mask:0xf bank_mask:0xf\n"
        "v_mfma_f32_16x16x32_bf16 v[24:27], v[20:23], a[224:227], v[24:27]\n"
        "v_and_b32_dpp v18, v47, v42 row_newbcast:12 row_mask:0xf bank_mask:0xf\n"
        "v_and_b32_dpp v19, v47, v43 row_newbcast:12 row_mask:0xf bank_mask:0xf\n"
        "v_mfma_f32_16x16x32_bf16 v[28:31], v[20:23], a[228:231], v[28:31]\n"
        "s_nop 1\n"
        "s_waitcnt lgkmcnt(1)\n"
        "v_mfma_f32_16x16x32_bf16 v[24:27], v[16:19], v[32:35], v[24:27]\n"
        "s_waitcnt lgkmcnt(0)\n"
        "v_mfma_f32_16x16x32_bf16 v[28:31], v[16:19], v[36:39], v[28:31]\n"
        "s_add_u32 s84, s84, 406\n"
        "s_add_u32 s50, s49, s12\n"
        "buffer_load_dwordx4 a[40:43], v0, s[36:39], s50 offen nt\n"
        "buffer_load_dwordx4 a[44:47], v0, s[36:39], s50 offen offset:256 nt\n"
        "s_branch .Lst_epi_%=\n"
        ".Lst_pv1_%=:\n"
        "v_add_u32 v5, s54, v2\n"
        "ds_read_b128 v[32:35], v3 offset:8192\n"
        "ds_read_b128 v[36:39], v3 offset:9216\n"
        "s_nop 1\n"
        "v_and_b32_dpp v16, v44, v40 row_newbcast:0 row_mask:0xf bank_mask:0xf\n"
        "v_and_b32_dpp v17, v44, v41 row_newbcast:0 row_mask:0xf bank_mask:0xf\n"
        "v_and_b32_dpp v18, v44, v42 row_newbcast:0 row_mask:0xf bank_mask:0xf\n"
        "v_and_b32_dpp v19, v44, v43 row_newbcast:0 row_mask:0xf bank_mask:0xf\n"
        "v_and_b32_dpp v20, v45, v40 row_newbcast:0 row_mask:0xf bank_mask:0xf\n"
        "v_and_b32_dpp v21, v45, v41 row_newbcast:0 row_mask:0xf bank_mask:0xf\n"
        "v_mfma_f32_16x16x32_bf16 v[24:27], v[16:19], v[144:147], 0\n"
        "v_and_b32_dpp v22, v45, v42 row_newbcast:0 row_mask:0xf bank_mask:0xf\n"
        "v_and_b32_dpp v23, v45, v43 row_newbcast:0 row_mask:0xf bank_mask:0xf\n"
        "v_mfma_f32_16x16x32_bf16 v[28:31], v[16:19], v[148:151], 0\n"
        "v_and_b32_dpp v16, v46, v40 row_newbcast:0 row_mask:0xf bank_mask:0xf\n"
        "v_and_b32_dpp v17, v46, v41 row_newbcast:0 row_mask:0xf bank_mask:0xf\n"
        "v_mfma_f32_16x16x32_bf16 v[24:27], v[20:23], v[152:155], v[24:27]\n"
        "v_and_b32_dpp v18, v46, v42 row_newbcast:0 row_mask:0xf bank_mask:0xf\n"
        "v_and_b32_dpp v19, v46, v43 row_newbcast:0 row_mask:0xf bank_mask:0xf\n"
        "v_mfma_f32_16x16x32_bf16 v[28:31], v[20:23], v[156:159], v[28:31]\n"
        "v_and_b32_dpp v20, v47, v40 row_newbcast:0 row_mask:0xf bank_mask:0xf\n"
        "v_and_b32_dpp v21, v47, v41 row_newbcast:0 row_mask:0xf bank_mask:0xf\n"
        "v_mfma_f32_16x16x32_bf16 v[24:27], v[16:19], v[160:163], v[24:27]\n"
        "v_and_b32_dpp v22, v47, v42 row_newbcast:0 row_mask:0xf bank_mask:0xf\n"
        "v_and_b32_dpp v23, v47, v43 row_newbcast:0 row_mask:0xf bank_mask:0xf\n"
        "v_mfma_f32_16x16x32_bf16 v[28:31], v[16:19], v[164:167], v[28:31]\n"
        "v_and_b32_dpp v16, v7, v40 row_newbcast:0 row_mask:0xf bank_mask:0xf\n"
        "v_and_b32_dpp v17, v7, v41 row_newbcast:0 row_mask:0xf bank_mask:0xf\n"
        "s_waitcnt lgkmcnt(1)\n"
        "v_mfma_f32_16x16x32_bf16 v[24:27], v[20:23], v[32:35], v[24:27]\n"
        "v_and_b32_dpp v18, v7, v42 row_newbcast:0 row_mask:0xf bank_mask:0xf\n"
        "v_and_b32_dpp v19, v7, v43 row_newbcast:0 row_mask:0xf bank_mask:0xf\n"
        "s_waitcnt lgkmcnt(0)\n"
        "v_mfma_f32_16x16x32_bf16 v[28:31], v[20:23], v[36:39], v[28:31]\n"
        "ds_read_b128 v[32:35], v3 offset:10240\n"
        "ds_read_b128 v[36:39], v3 offset:11264\n"
        "v_and_b32_dpp v20, v44, v40 row_newbcast:4 row_mask:0xf bank_mask:0xf\n"
        "v_and_b32_dpp v21, v44, v41 row_newbcast:4 row_mask:0xf bank_mask:0xf\n"
        "v_mfma_f32_16x16x32_bf16 v[24:27], v[16:19], v[168:171], v[24:27]\n"
        "v_and_b32_dpp v22, v44, v42 row_newbcast:4 row_mask:0xf bank_mask:0xf\n"
        "v_and_b32_dpp v23, v44, v43 row_newbcast:4 row_mask:0xf bank_mask:0xf\n"
        "v_mfma_f32_16x16x32_bf16 v[28:31], v[16:19], v[172:175], v[28:31]\n"
        "v_and_b32_dpp v16, v45, v40 row_newbcast:4 row_mask:0xf bank_mask:0xf\n"
        "v_and_b32_dpp v17, v45, v41 row_newbcast:4 row_mask:0xf bank_mask:0xf\n"
        "v_mfma_f32_16x16x32_bf16 v[24:27], v[20:23], v[176:179], v[24:27]\n"
        "v_and_b32_dpp v18, v45, v42 row_newbcast:4 row_mask:0xf bank_mask:0xf\n"
        "v_and_b32_dpp v19, v45, v43 row_newbcast:4 row_mask:0xf bank_mask:0xf\n"
        "v_mfma_f32_16x16x32_bf16 v[28:31], v[20:23], v[180:183], v[28:31]\n"
        "v_and_b32_dpp v20, v46, v40 row_newbcast:4 row_mask:0xf bank_mask:0xf\n"
        "v_and_b32_dpp v21, v46, v41 row_newbcast:4 row_mask:0xf bank_mask:0xf\n"
        "v_mfma_f32_16x16x32_bf16 v[24:27], v[16:19], v[184:187], v[24:27]\n"
        "v_and_b32_dpp v22, v46, v42 row_newbcast:4 row_mask:0xf bank_mask:0xf\n"
        "v_and_b32_dpp v23, v46, v43 row_newbcast:4 row_mask:0xf bank_mask:0xf\n"
        "v_mfma_f32_16x16x32_bf16 v[28:31], v[16:19], v[188:191], v[28:31]\n"
        "v_and_b32_dpp v16, v47, v40 row_newbcast:4 row_mask:0xf bank_mask:0xf\n"
        "v_and_b32_dpp v17, v47, v41 row_newbcast:4 row_mask:0xf bank_mask:0xf\n"
        "s_waitcnt lgkmcnt(1)\n"
        "v_mfma_f32_16x16x32_bf16 v[24:27], v[20:23], v[32:35], v[24:27]\n"
        "v_and_b32_dpp v18, v47, v42 row_newbcast:4 row_mask:0xf bank_mask:0xf\n"
        "v_and_b32_dpp v19, v47, v43 row_newbcast:4 row_mask:0xf bank_mask:0xf\n"
        "s_waitcnt lgkmcnt(0)\n"
        "v_mfma_f32_16x16x32_bf16 v[28:31], v[20:23], v[36:39], v[28:31]\n"
        "ds_read_b128 v[32:35], v3 offset:12288\n"
        "ds_read_b128 v[36:39], v3 offset:13312\n"
        "v_and_b32_dpp v20, v7, v40 row_newbcast:4 row_mask:0xf bank_mask:0xf\n"
        "v_and_b32_dpp v21, v7, v41 row_newbcast:4 row_mask:0xf bank_mask:0xf\n"
        "v_mfma_f32_16x16x32_bf16 v[24:27], v[16:19], v[192:195], v[24:27]\n"
        "v_and_b32_dpp v22, v7, v42 row_newbcast:4 row_mask:0xf bank_mask:0xf\n"
        "v_and_b32_dpp v23, v7, v43 row_newbcast:4 row_mask:0xf bank_mask:0xf\n"
        "v_mfma_f32_16x16x32_bf16 v[28:31], v[16:19], v[196:199], v[28:31]\n"
        "s_add_u32 s84, s84, 406\n"
        "s_add_u32 s50, s49, s11\n"
        "buffer_load_dwordx4 v[56:59], v0, s[36:39], s50 offen nt\n"
        "buffer_load_dwordx4 v[60:63], v0, s[36:39], s50 offen offset:256 nt\n"
        "v_and_b32_dpp v16, v44, v40 row_newbcast:8 row_mask:0xf bank_mask:0xf\n"
        "v_and_b32_dpp v17, v44, v41 row_newbcast:8 row_mask:0xf bank_mask:0xf\n"
        "v_mfma_f32_16x16x32_bf16 v[24:27], v[20:23], v[200:203], v[24:27]\n"
        "v_and_b32_dpp v18, v44, v42 row_newbcast:8 row_mask:0xf bank_mask:0xf\n"
        "v_and_b32_dpp v19, v44, v43 row_newbcast:8 row_mask:0xf bank_mask:0xf\n"
        "v_mfma_f32_16x16x32_bf16 v[28:31], v[20:23], v[204:207], v[28:31]\n"
        "v_and_b32_dpp v20, v45, v40 row_newbcast:8 row_mask:0xf bank_mask:0xf\n"
        "v_and_b32_dpp v21, v45, v41 row_newbcast:8 row_mask:0xf bank_mask:0xf\n"
        "v_mfma_f32_16x16x32_bf16 v[24:27], v[16:19], v[208:211], v[24:27]\n"
        "v_and_b32_dpp v22, v45, v42 row_newbcast:8 row_mask:0xf bank_mask:0xf\n"
        "v_and_b32_dpp v23, v45, v43 row_newbcast:8 row_mask:0xf bank_mask:0xf\n"
        "v_mfma_f32_16x16x32_bf16 v[28:31], v[16:19], v[212:215], v[28:31]\n"
        "v_and_b32_dpp v16, v46, v40 row_newbcast:8 row_mask:0xf bank_mask:0xf\n"
        "v_and_b32_dpp v17, v46, v41 row_newbcast:8 row_mask:0xf bank_mask:0xf\n"
        "s_waitcnt lgkmcnt(1)\n"
        "v_mfma_f32_16x16x32_bf16 v[24:27], v[20:23], v[32:35], v[24:27]\n"
        "v_and_b32_dpp v18, v46, v42 row_newbcast:8 row_mask:0xf bank_mask:0xf\n"
        "v_and_b32_dpp v19, v46, v43 row_newbcast:8 row_mask:0xf bank_mask:0xf\n"
        "s_waitcnt lgkmcnt(0)\n"
        "v_mfma_f32_16x16x32_bf16 v[28:31], v[20:23], v[36:39], v[28:31]\n"
        "ds_read_b128 v[32:35], v3 offset:14336\n"
        "ds_read_b128 v[36:39], v3 offset:15360\n"
        "v_and_b32_dpp v20, v47, v40 row_newbcast:8 row_mask:0xf bank_mask:0xf\n"
        "v_and_b32_dpp v21, v47, v41 row_newbcast:8 row_mask:0xf bank_mask:0xf\n"
        "v_mfma_f32_16x16x32_bf16 v[24:27], v[16:19], v[216:219], v[24:27]\n"
        "v_and_b32_dpp v22, v47, v42 row_newbcast:8 row_mask:0xf bank_mask:0xf\n"
        "v_and_b32_dpp v23, v47, v43 row_newbcast:8 row_mask:0xf bank_mask:0xf\n"
        "v_mfma_f32_16x16x32_bf16 v[28:31], v[16:19], v[220:223], v[28:31]\n"
        "v_and_b32_dpp v16, v7, v40 row_newbcast:8 row_mask:0xf bank_mask:0xf\n"
        "v_and_b32_dpp v17, v7, v41 row_newbcast:8 row_mask:0xf bank_mask:0xf\n"
        "v_mfma_f32_16x16x32_bf16 v[24:27], v[20:23], v[224:227], v[24:27]\n"
        "v_and_b32_dpp v18, v7, v42 row_newbcast:8 row_mask:0xf bank_mask:0xf\n"
        "v_and_b32_dpp v19, v7, v43 row_newbcast:8 row_mask:0xf bank_mask:0xf\n"
        "v_mfma_f32_16x16x32_bf16 v[28:31], v[20:23], v[228:231], v[28:31]\n"
        "v_and_b32_dpp v20, v44, v40 row_newbcast:12 row_mask:0xf bank_mask:0xf\n"
        "v_and_b32_dpp v21, v44, v41 row_newbcast:12 row_mask:0xf bank_mask:0xf\n"
        "v_mfma_f32_16x16x32_bf16 v[24:27], v[16:19], v[232:235], v[24:27]\n"
        "v_and_b32_dpp v22, v44, v42 row_newbcast:12 row_mask:0xf bank_mask:0xf\n"
        "v_and_b32_dpp v23, v44, v43 row_newbcast:12 row_mask:0xf bank_mask:0xf\n"
        "v_mfma_f32_16x16x32_bf16 v[28:31], v[16:19], v[236:239], v[28:31]\n"
        "v_and_b32_dpp v16, v45, v40 row_newbcast:12 row_mask:0xf bank_mask:0xf\n"
        "v_and_b32_dpp v17, v45, v41 row_newbcast:12 row_mask:0xf bank_mask:0xf\n"
        "s_waitcnt lgkmcnt(1)\n"
        "v_mfma_f32_16x16x32_bf16 v[24:27], v[20:23], v[32:35], v[24:27]\n"
        "v_and_b32_dpp v18, v45, v42 row_newbcast:12 row_mask:0xf bank_mask:0xf\n"
        "v_and_b32_dpp v19, v45, v43 row_newbcast:12 row_mask:0xf bank_mask:0xf\n"
        "s_waitcnt lgkmcnt(0)\n"
        "v_mfma_f32_16x16x32_bf16 v[28:31], v[20:23], v[36:39], v[28:31]\n"
        "ds_read_b128 v[32:35], v5 offset:8192\n"
        "ds_read_b128 v[36:39], v5 offset:9216\n"
        "v_and_b32_dpp v20, v46, v40 row_newbcast:12 row_mask:0xf bank_mask:0xf\n"
        "v_and_b32_dpp v21, v46, v41 row_newbcast:12 row_mask:0xf bank_mask:0xf\n"
        "v_mfma_f32_16x16x32_bf16 v[24:27], v[16:19], v[240:243], v[24:27]\n"
        "v_and_b32_dpp v22, v46, v42 row_newbcast:12 row_mask:0xf bank_mask:0xf\n"
        "v_and_b32_dpp v23, v46, v43 row_newbcast:12 row_mask:0xf bank_mask:0xf\n"
        "v_mfma_f32_16x16x32_bf16 v[28:31], v[16:19], v[244:247], v[28:31]\n"
        "v_and_b32_dpp v16, v47, v40 row_newbcast:12 row_mask:0xf bank_mask:0xf\n"
        "v_and_b32_dpp v17, v47, v41 row_newbcast:12 row_mask:0xf bank_mask:0xf\n"
        "v_mfma_f32_16x16x32_bf16 v[24:27], v[20:23], v[248:251], v[24:27]\n"
        "v_and_b32_dpp v18, v47, v42 row_newbcast:12 row_mask:0xf bank_mask:0xf\n"
        "v_and_b32_dpp v19, v47, v43 row_newbcast:12 row_mask:0xf bank_mask:0xf\n"
        "v_mfma_f32_16x16x32_bf16 v[28:31], v[20:23], v[252:255], v[28:31]\n"
        "s_nop 1\n"
        "s_waitcnt lgkmcnt(1)\n"
        "v_mfma_f32_16x16x32_bf16 v[24:27], v[16:19], v[32:35], v[24:27]\n"
        "s_waitcnt lgkmcnt(0)\n"
        "v_mfma_f32_16x16x32_bf16 v[28:31], v[16:19], v[36:39], v[28:31]\n"
        "s_add_u32 s84, s84, 406\n"
        "s_add_u32 s50, s49, s12\n"
        "buffer_load_dwordx4 v[64:67], v0, s[36:39], s50 offen nt\n"
        "buffer_load_dwordx4 v[68:71], v0, s[36:39], s50 offen offset:256 nt\n"
        ".Lst_epi_%=:\n"
        "v_and_b32 v32, 0xffff0000, v44\n"
        "v_and_b32 v33, 0xffff0000, v45\n"
        "v_and_b32 v34, 0xffff0000, v46\n"
        "v_and_b32 v35, 0xffff0000, v47\n"
        "v_and_b32 v36, 0xffff0000, v7\n"
        "v_add_f32 v32, v32, v33\n"
        "v_add_f32 v34, v34, v35\n"
        "v_add_f32 v32, v32, v36\n"
        "v_add_f32 v33, v32, v34\n"
        "v_mov_b32 v32, s58\n"
        "s_nop 0\n"
        "v_add_f32_dpp v33, v33, v33 row_ror:4 row_mask:0xf bank_mask:0xf\n"
        "s_mov_b32 exec_hi, 0\n"
        "ds_write_b128 v4, v[24:27] offset:0\n"
        "ds_write_b128 v4, v[28:31] offset:512\n"
        "s_mov_b32 exec_hi, -1\n"
        "v_add_f32_dpp v33, v33, v33 row_ror:8 row_mask:0xf bank_mask:0xf\n"
        "s_add_u32 s59, s74, 159744\n"
        "v_mov_b32 v34, s59\n"
        "v_add_f32_dpp v33, v33, v33 row_bcast:15 row_mask:0xa bank_mask:0xf\n"
        "v_mov_b32 v35, 159744\n"
        "v_mov_b32 v36, 1\n"
        "v_add_f32_dpp v33, v33, v33 row_bcast:31 row_mask:0xc bank_mask:0xf\n"
        "v_add_u32 v37, 155648, v2\n"
        "s_mov_b32 exec_lo, 0\n"
        "s_mov_b32 exec_hi, 0x80000000\n"
        "ds_write_b64 v34, v[32:33]\n"
        "ds_add_rtn_u32 v5, v35, v36 offset:32\n"
        "s_mov_b64 exec, -1\n"
        "ds_read_b128 v[16:19], v37 offset:0\n"
        "ds_read_b128 v[20:23], v37 offset:1024\n"
        "ds_read_b128 v[24:27], v37 offset:2048\n"
        "ds_read_b128 v[28:31], v37 offset:3072\n"
        "ds_read_b128 v[36:39], v35 offset:16\n"
        "ds_read_b128 v[32:35], v35\n"
        "s_waitcnt lgkmcnt(6)\n"
        "v_readlane_b32 s59, v5, 63\n"
        "s_and_b32 s59, s59, 3\n"
        "s_cmp_eq_u32 s59, 3\n"
        "s_waitcnt lgkmcnt(0)\n"
        "s_cbranch_scc0 .Lst_epi_done_%=\n"
        "v_max3_f32 v5, v32, v34, v36\n"
        "v_max_f32 v5, v5, v38\n"
        "v_ceil_f32 v5, v5\n"
        "v_sub_f32 v32, v32, v5\n"
        "v_sub_f32 v34, v34, v5\n"
        "v_sub_f32 v36, v36, v5\n"
        "v_sub_f32 v38, v38, v5\n"
        "v_exp_f32 v32, v32\n"
        "v_exp_f32 v34, v34\n"
        "v_exp_f32 v36, v36\n"
        "v_exp_f32 v38, v38\n"
        "s_nop 0\n"
        "v_mul_f32 v16, v32, v16\n"
        "v_mul_f32 v17, v32, v17\n"
        "v_mul_f32 v18, v32, v18\n"
        "v_mul_f32 v19, v32, v19\n"
        "v_fmac_f32 v16, v34, v20\n"
        "v_fmac_f32 v17, v34, v21\n"
        "v_fmac_f32 v18, v34, v22\n"
        "v_fmac_f32 v19, v34, v23\n"
        "v_fmac_f32 v16, v36, v24\n"
        "v_fmac_f32 v17, v36, v25\n"
        "v_fmac_f32 v18, v36, v26\n"
        "v_fmac_f32 v19, v36, v27\n"
        "v_fmac_f32 v16, v38, v28\n"
        "v_fmac_f32 v17, v38, v29\n"
        "v_fmac_f32 v18, v38, v30\n"
        "v_fmac_f32 v19, v38, v31\n"
        "v_lshrrev_b32 v6, 1, v2\n"
        "v_cvt_pk_bf16_f32 v16, v16, v17\n"
        "v_min_u32 v16, -2, v16\n"
        "v_cvt_pk_bf16_f32 v17, v18, v19\n"
        "v_min_u32 v17, -2, v17\n"
        "buffer_store_dwordx2 v[16:17], v6, s[44:47], s64 offen sc1\n"
        "v_mul_f32 v33, v32, v33\n"
        "v_fmac_f32 v33, v34, v35\n"
        "v_fmac_f32 v33, v36, v37\n"
        "v_fmac_f32 v33, v38, v39\n"
        "v_min_u32 v32, -2, v5\n"
        "v_min_u32 v33, -2, v33\n"
        "s_mov_b64 exec, 0xffff\n"
        "buffer_store_dwordx2 v[32:33], v6, s[44:47], s64 offen offset:512 sc1\n"
        "s_mov_b64 exec, -1\n"
        "s_nop 0\n"
        ".Lst_epi_done_%=:\n"
        "s_setpc_b64 s[66:67]\n"
        :
        : "s"(Kc), "s"(Vc), "s"(ws), "s"(base), "s"(has19), "s"(w), "s"(lopt), "s"(qoff), "s"(poff), "s"(WS_BYTES), "s"(dbg), "s"(lds0), "s"(rdy),
          "s"(wq), "s"(wo), "s"(wg), "s"(wu), "s"(wd), "s"(xg), "s"(si * 4 + w), "s"(__builtin_amdgcn_readfirstlane((h * 1) % 18)), "s"(opto)
        : "memory", "vcc", "scc",
          "s35", "s36", "s37", "s38", "s39", "s40", "s41", "s42", "s43", "s44", "s45", "s46", "s47", "s48", "s49", "s50", "s51", "s52", "s53", "s54", "s55", "s56", "s57", "s58",
          "s59", "s60", "s61", "s62", "s63", "s64", "s65", "s66", "s67", "s68", "s69", "s70", "s71", "s72", "s73", "s74", "s75", "s76", "s77", "s78", "s79", "s80", "s81", "s82",
          "s83", "s84", "s85", "s86", "s87", "s88", "s89", "s90", "s91", "s92", "s93", "s94", "s95", "s96", "s97", "s98", "s99", "v255", "a255"
    );
    __builtin_unreachable();
  }
  const int c = 4 * xg + bi;
  const int dbg = DBG_OFF + 1024 * 512 + (c * 4 + w) * 256;
  asm volatile(
        "s_memrealtime s[68:69]\n"
        "s_load_dword s35, %7, 0\n"
        "s_mov_b64 exec, -1\n"
        "s_mov_b32 s48, %8\n"
        "s_mov_b32 s49, %9\n"
        "s_mov_b32 s43, 0x20000\n"
        "s_mov_b32 s47, 0x20000\n"
        "v_mbcnt_lo_u32_b32 v24, -1, 0\n"
        "v_mbcnt_hi_u32_b32 v24, -1, v24\n"
        "v_and_b32 v25, 15, v24\n"
        "v_lshrrev_b32 v26, 4, v24\n"
        "s_and_b32 s53, s49, 1\n"
        "s_lshl_b32 s53, s53, 3\n"
        "v_lshlrev_b32 v27, 1, v26\n"
        "v_xor_b32 v27, v27, v25\n"
        "v_xor_b32 v27, s53, v27\n"
        "v_lshlrev_b32 v27, 4, v27\n"
        "v_lshl_add_u32 v30, v26, 13, v27\n"
        "s_mov_b64 s[40:41], %1\n"
        "s_and_b32 s41, s41, 0xffff\n"
        "s_mov_b32 s42, 3670016\n"
        "s_lshl_b32 s57, s48, 8\n"
        "s_lshl_b32 s52, s49, 16\n"
        "s_add_u32 s57, s57, s52\n"
        "s_lshl_b32 s59, s49, 11\n"
        "s_add_u32 m0, s59, 32768\n"
        "s_add_u32 s52, s57, 0\n"
        "buffer_load_dwordx4 v30, s[40:43], s52 offen lds\n"
        "s_add_u32 m0, s59, 33792\n"
        "s_add_u32 s52, s57, 32768\n"
        "buffer_load_dwordx4 v30, s[40:43], s52 offen lds\n"
        "s_add_u32 m0, s59, 40960\n"
        "s_add_u32 s52, s57, 262144\n"
        "buffer_load_dwordx4 v30, s[40:43], s52 offen lds\n"
        "s_add_u32 m0, s59, 41984\n"
        "s_add_u32 s52, s57, 294912\n"
        "buffer_load_dwordx4 v30, s[40:43], s52 offen lds\n"
        "s_add_u32 m0, s59, 49152\n"
        "s_add_u32 s52, s57, 524288\n"
        "buffer_load_dwordx4 v30, s[40:43], s52 offen lds\n"
        "s_add_u32 m0, s59, 50176\n"
        "s_add_u32 s52, s57, 557056\n"
        "buffer_load_dwordx4 v30, s[40:43], s52 offen lds\n"
        "s_add_u32 m0, s59, 57344\n"
        "s_add_u32 s52, s57, 786432\n"
        "buffer_load_dwordx4 v30, s[40:43], s52 offen lds\n"
        "s_add_u32 m0, s59, 58368\n"
        "s_add_u32 s52, s57, 819200\n"
        "buffer_load_dwordx4 v30, s[40:43], s52 offen lds\n"
        "s_add_u32 m0, s59, 65536\n"
        "s_add_u32 s52, s57, 1048576\n"
        "buffer_load_dwordx4 v30, s[40:43], s52 offen lds\n"
        "s_add_u32 m0, s59, 66560\n"
        "s_add_u32 s52, s57, 1081344\n"
        "buffer_load_dwordx4 v30, s[40:43], s52 offen lds\n"
        "s_add_u32 m0, s59, 73728\n"
        "s_add_u32 s52, s57, 1310720\n"
        "buffer_load_dwordx4 v30, s[40:43], s52 offen lds\n"
        "s_add_u32 m0, s59, 74752\n"
        "s_add_u32 s52, s57, 1343488\n"
        "buffer_load_dwordx4 v30, s[40:43], s52 offen lds\n"
        "s_add_u32 m0, s59, 81920\n"
        "s_add_u32 s52, s57, 1572864\n"
        "buffer_load_dwordx4 v30, s[40:43], s52 offen lds\n"
        "s_add_u32 m0, s59, 82944\n"
        "s_add_u32 s52, s57, 1605632\n"
        "buffer_load_dwordx4 v30, s[40:43], s52 offen lds\n"
        "s_add_u32 m0, s59, 90112\n"
        "s_add_u32 s52, s57, 1835008\n"
        "buffer_load_dwordx4 v30, s[40:43], s52 offen lds\n"
        "s_add_u32 m0, s59, 91136\n"
        "s_add_u32 s52, s57, 1867776\n"
        "buffer_load_dwordx4 v30, s[40:43], s52 offen lds\n"
        "s_add_u32 m0, s59, 98304\n"
        "s_add_u32 s52, s57, 2097152\n"
        "buffer_load_dwordx4 v30, s[40:43], s52 offen lds\n"
        "s_add_u32 m0, s59, 99328\n"
        "s_add_u32 s52, s57, 2129920\n"
        "buffer_load_dwordx4 v30, s[40:43], s52 offen lds\n"
        "s_add_u32 m0, s59, 106496\n"
        "s_add_u32 s52, s57, 2359296\n"
        "buffer_load_dwordx4 v30, s[40:43], s52 offen lds\n"
        "s_add_u32 m0, s59, 107520\n"
        "s_add_u32 s52, s57, 2392064\n"
        "buffer_load_dwordx4 v30, s[40:43], s52 offen lds\n"
        "s_add_u32 m0, s59, 114688\n"
        "s_add_u32 s52, s57, 2621440\n"
        "buffer_load_dwordx4 v30, s[40:43], s52 offen lds\n"
        "s_add_u32 m0, s59, 115712\n"
        "s_add_u32 s52, s57, 2654208\n"
        "buffer_load_dwordx4 v30, s[40:43], s52 offen lds\n"
        "s_add_u32 m0, s59, 131072\n"
        "s_add_u32 s52, s57, 2883584\n"
        "buffer_load_dwordx4 v30, s[40:43], s52 offen lds\n"
        "s_add_u32 m0, s59, 132096\n"
        "s_add_u32 s52, s57, 2916352\n"
        "buffer_load_dwordx4 v30, s[40:43], s52 offen lds\n"
        "s_add_u32 m0, s59, 139264\n"
        "s_add_u32 s52, s57, 3145728\n"
        "buffer_load_dwordx4 v30, s[40:43], s52 offen lds\n"
        "s_add_u32 m0, s59, 140288\n"
        "s_add_u32 s52, s57, 3178496\n"
        "buffer_load_dwordx4 v30, s[40:43], s52 offen lds\n"
        "s_add_u32 m0, s59, 147456\n"
        "s_add_u32 s52, s57, 3407872\n"
        "buffer_load_dwordx4 v30, s[40:43], s52 offen lds\n"
        "s_add_u32 m0, s59, 148480\n"
        "s_add_u32 s52, s57, 3440640\n"
        "buffer_load_dwordx4 v30, s[40:43], s52 offen lds\n"
        "s_mov_b64 s[44:45], %0\n"
        "s_and_b32 s45, s45, 0xffff\n"
        "s_mov_b32 s46, 3584\n"
        "v_lshlrev_b32 v14, 4, v24\n"
        "v_min_u32 v14, 880, v14\n"
        "s_mul_i32 s56, s49, 896\n"
        "buffer_load_dwordx4 v[44:47], v14, s[44:47], s56 offen\n"
        "s_mov_b64 s[36:37], %7\n"
        "s_and_b32 s37, s37, 0xffff\n"
        "s_mov_b32 s38, 0\n"
        "s_mov_b32 s38, %10\n"
        "s_mov_b32 s39, 0x20000\n"
        "s_mov_b32 s80, %11\n"
        "s_mov_b32 s55, 0\n"
        "s_mov_b32 s81, 0x3f803f80\n"
        "s_sub_u32 s52, s48, 4\n"
        "s_cmp_lt_u32 s48, 28\n"
        "s_cselect_b32 s82, s48, s52\n"
        "v_mov_b32 v28, 1073741824\n"
        "v_lshrrev_b32 v5, 5, v24\n"
        "v_and_b32 v10, 31, v24\n"
        "v_lshlrev_b32 v27, 1, v5\n"
        "v_xor_b32 v27, v27, v10\n"
        "v_xor_b32 v27, s53, v27\n"
        "v_mul_u32_u24 v29, 896, v5\n"
        "v_cmp_gt_u32 vcc, 28, v27\n"
        "v_lshl_add_u32 v31, v27, 4, v29\n"
        "v_xor_b32 v27, 4, v27\n"
        "v_cndmask_b32 v31, v28, v31, vcc\n"
        "v_cmp_gt_u32 vcc, 28, v27\n"
        "v_lshl_add_u32 v32, v27, 4, v29\n"
        "v_xor_b32 v27, 4, v27\n"
        "v_cndmask_b32 v32, v28, v32, vcc\n"
        "v_and_b32 v27, 1, v26\n"
        "v_lshlrev_b32 v27, 1, v27\n"
        "v_and_b32 v29, 7, v24\n"
        "v_xor_b32 v27, v27, v29\n"
        "s_lshr_b32 s52, s53, 1\n"
        "v_xor_b32 v27, s52, v27\n"
        "v_lshrrev_b32 v29, 3, v24\n"
        "v_mul_u32_u24 v29, 3584, v29\n"
        "v_lshl_add_u32 v33, v27, 4, v29\n"
        "s_or_b32 s52, s53, 0\n"
        "v_xor_b32 v27, s52, v24\n"
        "v_cmp_gt_u32 vcc, 56, v27\n"
        "v_lshlrev_b32 v27, 4, v27\n"
        "s_nop 0\n"
        "v_cndmask_b32 v34, v28, v27, vcc\n"
        "s_or_b32 s52, s53, 2\n"
        "v_xor_b32 v27, s52, v24\n"
        "v_cmp_gt_u32 vcc, 56, v27\n"
        "v_lshlrev_b32 v27, 4, v27\n"
        "s_nop 0\n"
        "v_cndmask_b32 v35, v28, v27, vcc\n"
        "s_or_b32 s52, s53, 4\n"
        "v_xor_b32 v27, s52, v24\n"
        "v_cmp_gt_u32 vcc, 56, v27\n"
        "v_lshlrev_b32 v27, 4, v27\n"
        "s_nop 0\n"
        "v_cndmask_b32 v36, v28, v27, vcc\n"
        "s_or_b32 s52, s53, 6\n"
        "v_xor_b32 v27, s52, v24\n"
        "v_cmp_gt_u32 vcc, 56, v27\n"
        "v_lshlrev_b32 v27, 4, v27\n"
        "s_nop 0\n"
        "v_cndmask_b32 v37, v28, v27, vcc\n"
        "v_bfe_u32 v38, v24, 2, 3\n"
        "v_lshlrev_b32 v38, 5, v38\n"
        "v_bfe_u32 v92, v24, 2, 2\n"
        "v_lshl_add_u32 v92, v26, 3, v92\n"
        "v_and_b32 v93, 3, v24\n"
        "v_lshlrev_b32 v93, 3, v93\n"
        "v_lshl_add_u32 v27, v26, 5, v25\n"
        "v_lshlrev_b32 v27, 1, v27\n"
        "v_mov_b32 v29, 5376\n"
        "v_cmp_ne_u32 vcc, 3, v26\n"
        "s_mul_i32 s52, s49, 224\n"
        "v_add_u32 v8, s52, v27\n"
        "v_add_u32 v9, 32, v8\n"
        "v_cndmask_b32 v9, v29, v9, vcc\n"
        "v_and_b32 v27, 7, v25\n"
        "v_lshl_add_u32 v27, v27, 1, v5\n"
        "v_and_b32 v7, 1, v26\n"
        "v_lshl_add_u32 v27, v7, 5, v27\n"
        "v_lshrrev_b32 v7, 3, v25\n"
        "v_lshl_add_u32 v27, v7, 6, v27\n"
        "s_lshl_b32 s54, s53, 4\n"
        "v_add_u32 v27, s54, v27\n"
        "v_cmp_gt_u32 vcc, 224, v27\n"
        "s_lshr_b32 s52, s49, 1\n"
        "s_mul_i32 s52, s52, 224\n"
        "v_add_lshl_u32 v6, s52, v27, 1\n"
        "v_cndmask_b32 v6, v29, v6, vcc\n"
        "v_add_u32 v7, 32, v6\n"
        "v_lshlrev_b32 v27, 4, v25\n"
        "v_lshl_add_u32 v5, v26, 10, v27\n"
        "v_and_b32 v27, 1, v25\n"
        "v_lshlrev_b32 v27, 4, v27\n"
        "v_mov_b32 v29, 0x3f80\n"
        "v_lshlrev_b32 v27, v27, v29\n"
        "v_lshrrev_b32 v29, 1, v25\n"
        "v_cmp_eq_u32 vcc, 0, v29\n"
        "s_nop 1\n"
        "v_cndmask_b32 v11, 0, v27, vcc\n"
        "v_accvgpr_write_b32 a248, v11\n"
        "v_cmp_eq_u32 vcc, 2, v29\n"
        "s_nop 1\n"
        "v_cndmask_b32 v11, 0, v27, vcc\n"
        "v_accvgpr_write_b32 a249, v11\n"
        "v_cmp_eq_u32 vcc, 4, v29\n"
        "s_nop 1\n"
        "v_cndmask_b32 v11, 0, v27, vcc\n"
        "v_accvgpr_write_b32 a250, v11\n"
        "v_cmp_eq_u32 vcc, 6, v29\n"
        "s_nop 1\n"
        "v_cndmask_b32 v11, 0, v27, vcc\n"
        "v_accvgpr_write_b32 a251, v11\n"
        "v_lshrrev_b32 v27, 3, v25\n"
        "v_lshl_add_u32 v27, v27, 2, v26\n"
        "v_cmp_gt_u32 vcc, 6, v27\n"
        "v_mul_u32_u24 v1, 640, v27\n"
        "v_and_b32 v29, 7, v25\n"
        "v_lshl_add_u32 v1, v29, 4, v1\n"
        "s_lshl_b32 s52, s49, 7\n"
        "v_add_u32 v1, s52, v1\n"
        "v_cndmask_b32 v2, v28, v1, vcc\n"
        "v_and_b32 v0, 4, v25\n"
        "v_lshl_add_u32 v27, v0, 1, v27\n"
        "v_cmp_gt_u32 s[66:67], 14, v27\n"
        "v_mul_u32_u24 v27, 640, v27\n"
        "v_add_u32 v27, 512, v27\n"
        "v_cndmask_b32 v0, v28, v27, s[66:67]\n"
        "v_and_b32 v27, 1, v25\n"
        "v_lshlrev_b32 v27, 4, v27\n"
        "v_mov_b32 v220, 0xffff\n"
        "v_lshlrev_b32 v27, v27, v220\n"
        "v_lshrrev_b32 v29, 1, v29\n"
        "v_cmp_eq_u32 vcc, 0, v29\n"
        "s_nop 1\n"
        "v_cndmask_b32 v220, 0, v27, vcc\n"
        "v_cmp_eq_u32 vcc, 1, v29\n"
        "s_nop 1\n"
        "v_cndmask_b32 v221, 0, v27, vcc\n"
        "v_cmp_eq_u32 vcc, 2, v29\n"
        "s_nop 1\n"
        "v_cndmask_b32 v222, 0, v27, vcc\n"
        "v_cmp_eq_u32 vcc, 3, v29\n"
        "s_nop 1\n"
        "v_cndmask_b32 v223, 0, v27, vcc\n"
        "v_and_b32 v27, 1, v26\n"
        "v_lshlrev_b32 v27, 3, v27\n"
        "v_lshl_add_u32 v27, v25, 4, v27\n"
        "v_and_b32 v29, 2, v26\n"
        "v_lshl_add_u32 v27, v29, 1, v27\n"
        "v_add_u32 v27, s52, v27\n"
        "v_cmp_gt_u32 vcc, 8, v25\n"
        "v_mov_b32 v29, 1280\n"
        "s_mul_i32 s52, s49, 7168\n"
        "v_cndmask_b32 v3, v29, v27, vcc\n"
        "s_add_u32 s52, s52, 98304\n"
        "v_lshlrev_b32 v27, 4, v24\n"
        "v_add_u32 v218, s52, v27\n"
        "v_cmp_gt_u32 vcc, 16, v24\n"
        "v_and_b32 v29, 1, v24\n"
        "v_mul_u32_u24 v12, 30, v29\n"
        "v_lshlrev_b32 v27, 3, v24\n"
        "v_cndmask_b32 v10, v28, v27, vcc\n"
        "v_lshlrev_b32 v27, 4, v24\n"
        "v_cndmask_b32 v11, v28, v27, vcc\n"
        "v_lshl_add_u32 v12, v25, 1, v12\n"
        "v_cmp_gt_u32 vcc, 32, v24\n"
        "v_lshl_add_u32 v12, v26, 6, v12\n"
        "s_nop 0\n"
        "v_cndmask_b32 v12, v28, v12, vcc\n"
        "v_cmp_eq_u32 vcc, 0, v29\n"
        "v_mov_b32 v20, 0x03020706\n"
        "v_mov_b32 v27, 0x05040100\n"
        "v_cndmask_b32 v20, v20, v27, vcc\n"
        "v_mov_b32 v21, -2\n"
        "v_lshlrev_b32 v4, 4, v26\n"
        "s_lshl_b32 s52, s49, 5\n"
        "v_lshlrev_b32 v27, 1, v25\n"
        "v_add_u32 v13, s52, v27\n"
        "v_cmp_gt_u32 vcc, 56, v24\n"
        "v_mov_b32 v27, 0x3b124925\n"
        "s_nop 0\n"
        "v_cndmask_b32 v15, 0, v27, vcc\n"
        "s_lshr_b32 s60, s48, 1\n"
        "s_mul_i32 s60, s60, 8960\n"
        "s_add_u32 s60, s60, 327680\n"
        "s_lshl_b32 s61, s48, 9\n"
        "s_lshl_b32 s52, s49, 7\n"
        "s_add_u32 s61, s61, s52\n"
        "s_add_u32 s61, s61, 1474560\n"
        "s_lshl_b32 s52, s49, 8\n"
        "s_add_u32 s62, s52, 1474560\n"
        "s_add_u32 s64, s52, 1605632\n"
        "s_lshl_b32 s63, s48, 10\n"
        "s_add_u32 s63, s63, s64\n"
        "s_lshl_b32 s65, s48, 10\n"
        "s_lshl_b32 s52, s49, 8\n"
        "s_add_u32 s65, s65, s52\n"
        "s_add_u32 s65, s65, 65536\n"
        "v_mov_b32 v52, -1\n"
        "v_mov_b32 v53, -1\n"
        "v_mov_b32 v54, -1\n"
        "v_mov_b32 v55, -1\n"
        "v_lshrrev_b32 v27, 3, v24\n"
        "v_mul_u32_u24 v27, 16256, v27\n"
        "v_lshl_add_u32 v27, v24, 4, v27\n"
        "buffer_store_dwordx4 v[52:55], v27, s[36:39], s61 offen sc1\n"
        "v_mul_u32_u24 v27, 32768, v26\n"
        "v_lshl_add_u32 v27, v25, 4, v27\n"
        "s_add_u32 s52, s63, 0\n"
        "buffer_store_dwordx4 v[52:55], v27, s[36:39], s52 offen sc1\n"
        "s_add_u32 s52, s63, 131072\n"
        "buffer_store_dwordx4 v[52:55], v27, s[36:39], s52 offen sc1\n"
        "v_mul_u32_u24 v27, 32768, v26\n"
        "v_lshl_add_u32 v27, v25, 3, v27\n"
        "s_add_u32 s52, s65, 131072\n"
        "buffer_store_dwordx2 v[52:53], v27, s[36:39], s52 offen sc1\n"
        "s_waitcnt lgkmcnt(0)\n"
        "s_add_u32 s35, s35, 1\n"
        "s_max_u32 s35, s35, 1\n"
        "v_and_b32 v27, 3, v25\n"
        "v_mul_u32_u24 v27, 896, v27\n"
        "v_add_u32 v42, v27, v4\n"
        "v_add_u32 v27, s56, v14\n"
        "v_mov_b32 v48, s81\n"
        "v_mov_b32 v49, s81\n"
        "v_mov_b32 v50, s81\n"
        "v_mov_b32 v51, s81\n"
        "s_waitcnt vmcnt(25)\n"
        "s_barrier\n"
        "s_mul_i32 s52, s49, 64\n"
        "s_add_u32 s52, s52, 0\n"
        "v_xor_b32 v39, s52, v38\n"
        "v_lshl_add_u32 v39, v92, 8, v39\n"
        "s_mov_b32 s52, 32768\n"
        "v_add3_u32 v39, v39, v93, s52\n"
        "ds_read_b64_tr_b16 a[0:1], v39 offset:0\n"
        "ds_read_b64_tr_b16 a[2:3], v39 offset:1024\n"
        "ds_read_b64_tr_b16 a[4:5], v39 offset:8192\n"
        "ds_read_b64_tr_b16 a[6:7], v39 offset:9216\n"
        "ds_read_b64_tr_b16 a[8:9], v39 offset:16384\n"
        "ds_read_b64_tr_b16 a[10:11], v39 offset:17408\n"
        "ds_read_b64_tr_b16 a[12:13], v39 offset:24576\n"
        "ds_read_b64_tr_b16 a[14:15], v39 offset:25600\n"
        "s_mul_i32 s52, s49, 64\n"
        "s_add_u32 s52, s52, 32\n"
        "v_xor_b32 v39, s52, v38\n"
        "v_lshl_add_u32 v39, v92, 8, v39\n"
        "s_mov_b32 s52, 32768\n"
        "v_add3_u32 v39, v39, v93, s52\n"
        "ds_read_b64_tr_b16 a[56:57], v39 offset:0\n"
        "ds_read_b64_tr_b16 a[58:59], v39 offset:1024\n"
        "ds_read_b64_tr_b16 a[60:61], v39 offset:8192\n"
        "ds_read_b64_tr_b16 a[62:63], v39 offset:9216\n"
        "ds_read_b64_tr_b16 a[64:65], v39 offset:16384\n"
        "ds_read_b64_tr_b16 a[66:67], v39 offset:17408\n"
        "ds_read_b64_tr_b16 a[68:69], v39 offset:24576\n"
        "ds_read_b64_tr_b16 a[70:71], v39 offset:25600\n"
        "s_waitcnt lgkmcnt(0)\n"
        "v_mfma_f32_16x16x32_bf16 v[60:63], v[48:51], a[0:3], 0\n"
        "v_mfma_f32_16x16x32_bf16 v[64:67], v[48:51], a[56:59], 0\n"
        "v_mfma_f32_16x16x32_bf16 v[60:63], v[48:51], a[4:7], v[60:63]\n"
        "v_mfma_f32_16x16x32_bf16 v[64:67], v[48:51], a[60:63], v[64:67]\n"
        "v_mfma_f32_16x16x32_bf16 v[60:63], v[48:51], a[8:11], v[60:63]\n"
        "v_mfma_f32_16x16x32_bf16 v[64:67], v[48:51], a[64:67], v[64:67]\n"
        "v_mfma_f32_16x16x32_bf16 v[60:63], v[48:51], a[12:15], v[60:63]\n"
        "v_mfma_f32_16x16x32_bf16 v[64:67], v[48:51], a[68:71], v[64:67]\n"
        "s_waitcnt vmcnt(17)\n"
        "s_barrier\n"
        "s_mov_b64 s[40:41], %2\n"
        "s_and_b32 s41, s41, 0xffff\n"
        "s_mov_b32 s42, 3670016\n"
        "s_lshr_b32 s57, s48, 1\n"
        "s_mul_i32 s57, s57, 229376\n"
        "s_and_b32 s52, s48, 1\n"
        "s_mul_i32 s52, s52, 448\n"
        "s_add_u32 s57, s57, s52\n"
        "s_mul_i32 s52, s49, 7168\n"
        "s_add_u32 s57, s57, s52\n"
        "s_lshl_b32 s59, s49, 12\n"
        "s_add_u32 m0, s59, 32768\n"
        "s_add_u32 s52, s57, 0\n"
        "buffer_load_dwordx4 v31, s[40:43], s52 offen lds\n"
        "s_add_u32 m0, s59, 33792\n"
        "s_add_u32 s52, s57, 1792\n"
        "buffer_load_dwordx4 v32, s[40:43], s52 offen lds\n"
        "s_add_u32 m0, s59, 34816\n"
        "s_add_u32 s52, s57, 3584\n"
        "buffer_load_dwordx4 v31, s[40:43], s52 offen lds\n"
        "s_add_u32 m0, s59, 35840\n"
        "s_add_u32 s52, s57, 5376\n"
        "buffer_load_dwordx4 v32, s[40:43], s52 offen lds\n"
        "s_add_u32 m0, s59, 49152\n"
        "s_add_u32 s52, s57, 28672\n"
        "buffer_load_dwordx4 v31, s[40:43], s52 offen lds\n"
        "s_add_u32 m0, s59, 50176\n"
        "s_add_u32 s52, s57, 30464\n"
        "buffer_load_dwordx4 v32, s[40:43], s52 offen lds\n"
        "s_add_u32 m0, s59, 51200\n"
        "s_add_u32 s52, s57, 32256\n"
        "buffer_load_dwordx4 v31, s[40:43], s52 offen lds\n"
        "s_add_u32 m0, s59, 52224\n"
        "s_add_u32 s52, s57, 34048\n"
        "buffer_load_dwordx4 v32, s[40:43], s52 offen lds\n"
        "s_mul_i32 s52, s49, 64\n"
        "s_add_u32 s52, s52, 0\n"
        "v_xor_b32 v39, s52, v38\n"
        "v_lshl_add_u32 v39, v92, 8, v39\n"
        "s_mov_b32 s52, 65536\n"
        "v_add3_u32 v39, v39, v93, s52\n"
        "ds_read_b64_tr_b16 a[16:17], v39 offset:0\n"
        "ds_read_b64_tr_b16 a[18:19], v39 offset:1024\n"
        "ds_read_b64_tr_b16 a[20:21], v39 offset:8192\n"
        "ds_read_b64_tr_b16 a[22:23], v39 offset:9216\n"
        "ds_read_b64_tr_b16 a[24:25], v39 offset:16384\n"
        "ds_read_b64_tr_b16 a[26:27], v39 offset:17408\n"
        "ds_read_b64_tr_b16 a[28:29], v39 offset:24576\n"
        "ds_read_b64_tr_b16 a[30:31], v39 offset:25600\n"
        "s_mul_i32 s52, s49, 64\n"
        "s_add_u32 s52, s52, 32\n"
        "v_xor_b32 v39, s52, v38\n"
        "v_lshl_add_u32 v39, v92, 8, v39\n"
        "s_mov_b32 s52, 65536\n"
        "v_add3_u32 v39, v39, v93, s52\n"
        "ds_read_b64_tr_b16 a[72:73], v39 offset:0\n"
        "ds_read_b64_tr_b16 a[74:75], v39 offset:1024\n"
        "ds_read_b64_tr_b16 a[76:77], v39 offset:8192\n"
        "ds_read_b64_tr_b16 a[78:79], v39 offset:9216\n"
        "ds_read_b64_tr_b16 a[80:81], v39 offset:16384\n"
        "ds_read_b64_tr_b16 a[82:83], v39 offset:17408\n"
        "ds_read_b64_tr_b16 a[84:85], v39 offset:24576\n"
        "ds_read_b64_tr_b16 a[86:87], v39 offset:25600\n"
        "s_waitcnt lgkmcnt(0)\n"
        "v_mfma_f32_16x16x32_bf16 v[60:63], v[48:51], a[16:19], v[60:63]\n"
        "v_mfma_f32_16x16x32_bf16 v[64:67], v[48:51], a[72:75], v[64:67]\n"
        "v_mfma_f32_16x16x32_bf16 v[60:63], v[48:51], a[20:23], v[60:63]\n"
        "v_mfma_f32_16x16x32_bf16 v[64:67], v[48:51], a[76:79], v[64:67]\n"
        "v_mfma_f32_16x16x32_bf16 v[60:63], v[48:51], a[24:27], v[60:63]\n"
        "v_mfma_f32_16x16x32_bf16 v[64:67], v[48:51], a[80:83], v[64:67]\n"
        "v_mfma_f32_16x16x32_bf16 v[60:63], v[48:51], a[28:31], v[60:63]\n"
        "v_mfma_f32_16x16x32_bf16 v[64:67], v[48:51], a[84:87], v[64:67]\n"
        "s_waitcnt vmcnt(19)\n"
        "s_barrier\n"
        "s_add_u32 m0, s59, 65536\n"
        "s_add_u32 s52, s57, 57344\n"
        "buffer_load_dwordx4 v31, s[40:43], s52 offen lds\n"
        "s_add_u32 m0, s59, 66560\n"
        "s_add_u32 s52, s57, 59136\n"
        "buffer_load_dwordx4 v32, s[40:43], s52 offen lds\n"
        "s_add_u32 m0, s59, 67584\n"
        "s_add_u32 s52, s57, 60928\n"
        "buffer_load_dwordx4 v31, s[40:43], s52 offen lds\n"
        "s_add_u32 m0, s59, 68608\n"
        "s_add_u32 s52, s57, 62720\n"
        "buffer_load_dwordx4 v32, s[40:43], s52 offen lds\n"
        "s_add_u32 m0, s59, 81920\n"
        "s_add_u32 s52, s57, 86016\n"
        "buffer_load_dwordx4 v31, s[40:43], s52 offen lds\n"
        "s_add_u32 m0, s59, 82944\n"
        "s_add_u32 s52, s57, 87808\n"
        "buffer_load_dwordx4 v32, s[40:43], s52 offen lds\n"
        "s_add_u32 m0, s59, 83968\n"
        "s_add_u32 s52, s57, 89600\n"
        "buffer_load_dwordx4 v31, s[40:43], s52 offen lds\n"
        "s_add_u32 m0, s59, 84992\n"
        "s_add_u32 s52, s57, 91392\n"
        "buffer_load_dwordx4 v32, s[40:43], s52 offen lds\n"
        "s_mul_i32 s52, s49, 64\n"
        "s_add_u32 s52, s52, 0\n"
        "v_xor_b32 v39, s52, v38\n"
        "v_lshl_add_u32 v39, v92, 8, v39\n"
        "s_mov_b32 s52, 98304\n"
        "v_add3_u32 v39, v39, v93, s52\n"
        "ds_read_b64_tr_b16 a[32:33], v39 offset:0\n"
        "ds_read_b64_tr_b16 a[34:35], v39 offset:1024\n"
        "ds_read_b64_tr_b16 a[36:37], v39 offset:8192\n"
        "ds_read_b64_tr_b16 a[38:39], v39 offset:9216\n"
        "ds_read_b64_tr_b16 a[40:41], v39 offset:16384\n"
        "ds_read_b64_tr_b16 a[42:43], v39 offset:17408\n"
        "s_mul_i32 s52, s49, 64\n"
        "s_add_u32 s52, s52, 32\n"
        "v_xor_b32 v39, s52, v38\n"
        "v_lshl_add_u32 v39, v92, 8, v39\n"
        "s_mov_b32 s52, 98304\n"
        "v_add3_u32 v39, v39, v93, s52\n"
        "ds_read_b64_tr_b16 a[88:89], v39 offset:0\n"
        "ds_read_b64_tr_b16 a[90:91], v39 offset:1024\n"
        "ds_read_b64_tr_b16 a[92:93], v39 offset:8192\n"
        "ds_read_b64_tr_b16 a[94:95], v39 offset:9216\n"
        "ds_read_b64_tr_b16 a[96:97], v39 offset:16384\n"
        "ds_read_b64_tr_b16 a[98:99], v39 offset:17408\n"
        "s_waitcnt lgkmcnt(0)\n"
        "v_mfma_f32_16x16x32_bf16 v[60:63], v[48:51], a[32:35], v[60:63]\n"
        "v_mfma_f32_16x16x32_bf16 v[64:67], v[48:51], a[88:91], v[64:67]\n"
        "v_mfma_f32_16x16x32_bf16 v[60:63], v[48:51], a[36:39], v[60:63]\n"
        "v_mfma_f32_16x16x32_bf16 v[64:67], v[48:51], a[92:95], v[64:67]\n"
        "v_mfma_f32_16x16x32_bf16 v[60:63], v[48:51], a[40:43], v[60:63]\n"
        "v_mfma_f32_16x16x32_bf16 v[64:67], v[48:51], a[96:99], v[64:67]\n"
        "s_waitcnt vmcnt(21)\n"
        "s_waitcnt vmcnt(16)\n"
        "ds_write_b128 v27, v[44:47]\n"
        "s_waitcnt lgkmcnt(0)\n"
        "s_barrier\n"
        "s_cmp_lg_u32 s49, 0\n"
        "s_cbranch_scc1 .Lch_nordy_%=\n"
        "v_mov_b32 v28, s35\n"
        "v_not_b32 v29, v28\n"
        "s_lshl_b32 s52, s48, 7\n"
        "s_add_u32 s52, s52, 4096\n"
        "s_mov_b64 exec, 1\n"
        "buffer_store_dwordx2 v[28:29], off, s[36:39], s52 sc1\n"
        "s_mov_b64 exec, -1\n"
        ".Lch_nordy_%=:\n"
        "s_add_u32 m0, s59, 98304\n"
        "s_add_u32 s52, s57, 114688\n"
        "buffer_load_dwordx4 v31, s[40:43], s52 offen lds\n"
        "s_add_u32 m0, s59, 99328\n"
        "s_add_u32 s52, s57, 116480\n"
        "buffer_load_dwordx4 v32, s[40:43], s52 offen lds\n"
        "s_add_u32 m0, s59, 100352\n"
        "s_add_u32 s52, s57, 118272\n"
        "buffer_load_dwordx4 v31, s[40:43], s52 offen lds\n"
        "s_add_u32 m0, s59, 101376\n"
        "s_add_u32 s52, s57, 120064\n"
        "buffer_load_dwordx4 v32, s[40:43], s52 offen lds\n"
        "s_add_u32 m0, s59, 114688\n"
        "s_add_u32 s52, s57, 143360\n"
        "buffer_load_dwordx4 v31, s[40:43], s52 offen lds\n"
        "s_add_u32 m0, s59, 115712\n"
        "s_add_u32 s52, s57, 145152\n"
        "buffer_load_dwordx4 v32, s[40:43], s52 offen lds\n"
        "s_add_u32 m0, s59, 116736\n"
        "s_add_u32 s52, s57, 146944\n"
        "buffer_load_dwordx4 v31, s[40:43], s52 offen lds\n"
        "s_add_u32 m0, s59, 117760\n"
        "s_add_u32 s52, s57, 148736\n"
        "buffer_load_dwordx4 v32, s[40:43], s52 offen lds\n"
        "s_mul_i32 s52, s49, 64\n"
        "s_add_u32 s52, s52, 0\n"
        "v_xor_b32 v39, s52, v38\n"
        "v_lshl_add_u32 v39, v92, 8, v39\n"
        "s_mov_b32 s52, 131072\n"
        "v_add3_u32 v39, v39, v93, s52\n"
        "ds_read_b64_tr_b16 a[44:45], v39 offset:0\n"
        "ds_read_b64_tr_b16 a[46:47], v39 offset:1024\n"
        "ds_read_b64_tr_b16 a[48:49], v39 offset:8192\n"
        "ds_read_b64_tr_b16 a[50:51], v39 offset:9216\n"
        "ds_read_b64_tr_b16 a[52:53], v39 offset:16384\n"
        "ds_read_b64_tr_b16 a[54:55], v39 offset:17408\n"
        "s_mul_i32 s52, s49, 64\n"
        "s_add_u32 s52, s52, 32\n"
        "v_xor_b32 v39, s52, v38\n"
        "v_lshl_add_u32 v39, v92, 8, v39\n"
        "s_mov_b32 s52, 131072\n"
        "v_add3_u32 v39, v39, v93, s52\n"
        "ds_read_b64_tr_b16 a[100:101], v39 offset:0\n"
        "ds_read_b64_tr_b16 a[102:103], v39 offset:1024\n"
        "ds_read_b64_tr_b16 a[104:105], v39 offset:8192\n"
        "ds_read_b64_tr_b16 a[106:107], v39 offset:9216\n"
        "ds_read_b64_tr_b16 a[108:109], v39 offset:16384\n"
        "ds_read_b64_tr_b16 a[110:111], v39 offset:17408\n"
        "s_waitcnt lgkmcnt(0)\n"
        "v_mfma_f32_16x16x32_bf16 v[60:63], v[48:51], a[44:47], v[60:63]\n"
        "v_mfma_f32_16x16x32_bf16 v[64:67], v[48:51], a[100:103], v[64:67]\n"
        "v_mfma_f32_16x16x32_bf16 v[60:63], v[48:51], a[48:51], v[60:63]\n"
        "v_mfma_f32_16x16x32_bf16 v[64:67], v[48:51], a[104:107], v[64:67]\n"
        "v_mfma_f32_16x16x32_bf16 v[60:63], v[48:51], a[52:55], v[60:63]\n"
        "v_mfma_f32_16x16x32_bf16 v[64:67], v[48:51], a[108:111], v[64:67]\n"
        "ds_read_b128 v[96:99], v42 offset:0\n"
        "ds_read_b128 v[100:103], v42 offset:64\n"
        "ds_read_b128 v[104:107], v42 offset:128\n"
        "ds_read_b128 v[108:111], v42 offset:192\n"
        "ds_read_b128 v[112:115], v42 offset:256\n"
        "ds_read_b128 v[116:119], v42 offset:320\n"
        "ds_read_b128 v[120:123], v42 offset:384\n"
        "ds_read_b128 v[124:127], v42 offset:448\n"
        "ds_read_b128 v[128:131], v42 offset:512\n"
        "ds_read_b128 v[132:135], v42 offset:576\n"
        "ds_read_b128 v[136:139], v42 offset:640\n"
        "ds_read_b128 v[140:143], v42 offset:704\n"
        "ds_read_b128 v[144:147], v42 offset:768\n"
        "ds_read_b128 v[148:151], v42 offset:832\n"
        "ds_read_b128 v[68:71], v14 offset:0\n"
        "ds_read_b128 v[72:75], v14 offset:896\n"
        "ds_read_b128 v[76:79], v14 offset:1792\n"
        "ds_read_b128 v[80:83], v14 offset:2688\n"
        "s_waitcnt lgkmcnt(0)\n"
        "v_mfma_f32_16x16x32_bf16 v[52:55], v[96:99], a[0:3], 0\n"
        "v_dot2_f32_bf16 v84, v68, s81, 0\n"
        "v_dot2_f32_bf16 v86, v72, s81, 0\n"
        "v_dot2_f32_bf16 v88, v76, s81, 0\n"
        "v_dot2_f32_bf16 v90, v80, s81, 0\n"
        "v_mfma_f32_16x16x32_bf16 v[56:59], v[96:99], a[56:59], 0\n"
        "v_dot2_f32_bf16 v85, v68, v68, 0\n"
        "v_dot2_f32_bf16 v87, v72, v72, 0\n"
        "v_dot2_f32_bf16 v89, v76, v76, 0\n"
        "v_dot2_f32_bf16 v91, v80, v80, 0\n"
        "v_mfma_f32_16x16x32_bf16 v[52:55], v[100:103], a[4:7], v[52:55]\n"
        "v_dot2_f32_bf16 v84, v69, s81, v84\n"
        "v_dot2_f32_bf16 v86, v73, s81, v86\n"
        "v_dot2_f32_bf16 v88, v77, s81, v88\n"
        "v_dot2_f32_bf16 v90, v81, s81, v90\n"
        "v_mfma_f32_16x16x32_bf16 v[56:59], v[100:103], a[60:63], v[56:59]\n"
        "v_dot2_f32_bf16 v85, v69, v69, v85\n"
        "v_dot2_f32_bf16 v87, v73, v73, v87\n"
        "v_dot2_f32_bf16 v89, v77, v77, v89\n"
        "v_dot2_f32_bf16 v91, v81, v81, v91\n"
        "v_mfma_f32_16x16x32_bf16 v[52:55], v[104:107], a[8:11], v[52:55]\n"
        "v_dot2_f32_bf16 v84, v70, s81, v84\n"
        "v_dot2_f32_bf16 v86, v74, s81, v86\n"
        "v_dot2_f32_bf16 v88, v78, s81, v88\n"
        "v_dot2_f32_bf16 v90, v82, s81, v90\n"
        "v_mfma_f32_16x16x32_bf16 v[56:59], v[104:107], a[64:67], v[56:59]\n"
        "v_dot2_f32_bf16 v85, v70, v70, v85\n"
        "v_dot2_f32_bf16 v87, v74, v74, v87\n"
        "v_dot2_f32_bf16 v89, v78, v78, v89\n"
        "v_dot2_f32_bf16 v91, v82, v82, v91\n"
        "v_mfma_f32_16x16x32_bf16 v[52:55], v[108:111], a[12:15], v[52:55]\n"
        "v_dot2_f32_bf16 v84, v71, s81, v84\n"
        "v_dot2_f32_bf16 v86, v75, s81, v86\n"
        "v_dot2_f32_bf16 v88, v79, s81, v88\n"
        "v_dot2_f32_bf16 v90, v83, s81, v90\n"
        "v_mfma_f32_16x16x32_bf16 v[56:59], v[108:111], a[68:71], v[56:59]\n"
        "v_dot2_f32_bf16 v85, v71, v71, v85\n"
        "v_dot2_f32_bf16 v87, v75, v75, v87\n"
        "v_dot2_f32_bf16 v89, v79, v79, v89\n"
        "v_dot2_f32_bf16 v91, v83, v83, v91\n"
        "v_mfma_f32_16x16x32_bf16 v[52:55], v[112:115], a[16:19], v[52:55]\n"
        "v_mul_f32 v84, v15, v84\n"
        "v_mul_f32 v86, v15, v86\n"
        "v_mul_f32 v88, v15, v88\n"
        "v_mul_f32 v90, v15, v90\n"
        "v_mfma_f32_16x16x32_bf16 v[56:59], v[112:115], a[72:75], v[56:59]\n"
        "v_mul_f32 v85, v15, v85\n"
        "v_mul_f32 v87, v15, v87\n"
        "v_mul_f32 v89, v15, v89\n"
        "v_mul_f32 v91, v15, v91\n"
        "v_mfma_f32_16x16x32_bf16 v[52:55], v[116:119], a[20:23], v[52:55]\n"
        "v_add_f32_dpp v84, v84, v84 row_ror:1 row_mask:0xf bank_mask:0xf\n"
        "v_add_f32_dpp v86, v86, v86 row_ror:1 row_mask:0xf bank_mask:0xf\n"
        "v_add_f32_dpp v88, v88, v88 row_ror:1 row_mask:0xf bank_mask:0xf\n"
        "v_add_f32_dpp v90, v90, v90 row_ror:1 row_mask:0xf bank_mask:0xf\n"
        "v_mfma_f32_16x16x32_bf16 v[56:59], v[116:119], a[76:79], v[56:59]\n"
        "v_add_f32_dpp v85, v85, v85 row_ror:1 row_mask:0xf bank_mask:0xf\n"
        "v_add_f32_dpp v87, v87, v87 row_ror:1 row_mask:0xf bank_mask:0xf\n"
        "v_add_f32_dpp v89, v89, v89 row_ror:1 row_mask:0xf bank_mask:0xf\n"
        "v_add_f32_dpp v91, v91, v91 row_ror:1 row_mask:0xf bank_mask:0xf\n"
        "v_mfma_f32_16x16x32_bf16 v[52:55], v[120:123], a[24:27], v[52:55]\n"
        "v_add_f32_dpp v84, v84, v84 row_ror:2 row_mask:0xf bank_mask:0xf\n"
        "v_add_f32_dpp v86, v86, v86 row_ror:2 row_mask:0xf bank_mask:0xf\n"
        "v_add_f32_dpp v88, v88, v88 row_ror:2 row_mask:0xf bank_mask:0xf\n"
        "v_add_f32_dpp v90, v90, v90 row_ror:2 row_mask:0xf bank_mask:0xf\n"
        "v_mfma_f32_16x16x32_bf16 v[56:59], v[120:123], a[80:83], v[56:59]\n"
        "v_add_f32_dpp v85, v85, v85 row_ror:2 row_mask:0xf bank_mask:0xf\n"
        "v_add_f32_dpp v87, v87, v87 row_ror:2 row_mask:0xf bank_mask:0xf\n"
        "v_add_f32_dpp v89, v89, v89 row_ror:2 row_mask:0xf bank_mask:0xf\n"
        "v_add_f32_dpp v91, v91, v91 row_ror:2 row_mask:0xf bank_mask:0xf\n"
        "v_mfma_f32_16x16x32_bf16 v[52:55], v[124:127], a[28:31], v[52:55]\n"
        "v_add_f32_dpp v84, v84, v84 row_ror:4 row_mask:0xf bank_mask:0xf\n"
        "v_add_f32_dpp v86, v86, v86 row_ror:4 row_mask:0xf bank_mask:0xf\n"
        "v_add_f32_dpp v88, v88, v88 row_ror:4 row_mask:0xf bank_mask:0xf\n"
        "v_add_f32_dpp v90, v90, v90 row_ror:4 row_mask:0xf bank_mask:0xf\n"
        "v_mfma_f32_16x16x32_bf16 v[56:59], v[124:127], a[84:87], v[56:59]\n"
        "v_add_f32_dpp v85, v85, v85 row_ror:4 row_mask:0xf bank_mask:0xf\n"
        "v_add_f32_dpp v87, v87, v87 row_ror:4 row_mask:0xf bank_mask:0xf\n"
        "v_add_f32_dpp v89, v89, v89 row_ror:4 row_mask:0xf bank_mask:0xf\n"
        "v_add_f32_dpp v91, v91, v91 row_ror:4 row_mask:0xf bank_mask:0xf\n"
        "v_mfma_f32_16x16x32_bf16 v[52:55], v[128:131], a[32:35], v[52:55]\n"
        "v_add_f32_dpp v84, v84, v84 row_ror:8 row_mask:0xf bank_mask:0xf\n"
        "v_add_f32_dpp v86, v86, v86 row_ror:8 row_mask:0xf bank_mask:0xf\n"
        "v_add_f32_dpp v88, v88, v88 row_ror:8 row_mask:0xf bank_mask:0xf\n"
        "v_add_f32_dpp v90, v90, v90 row_ror:8 row_mask:0xf bank_mask:0xf\n"
        "v_mfma_f32_16x16x32_bf16 v[56:59], v[128:131], a[88:91], v[56:59]\n"
        "v_add_f32_dpp v85, v85, v85 row_ror:8 row_mask:0xf bank_mask:0xf\n"
        "v_add_f32_dpp v87, v87, v87 row_ror:8 row_mask:0xf bank_mask:0xf\n"
        "v_add_f32_dpp v89, v89, v89 row_ror:8 row_mask:0xf bank_mask:0xf\n"
        "v_add_f32_dpp v91, v91, v91 row_ror:8 row_mask:0xf bank_mask:0xf\n"
        "v_mfma_f32_16x16x32_bf16 v[52:55], v[132:135], a[36:39], v[52:55]\n"
        "v_add_f32_dpp v84, v84, v84 row_bcast:15 row_mask:0xa bank_mask:0xf\n"
        "v_add_f32_dpp v86, v86, v86 row_bcast:15 row_mask:0xa bank_mask:0xf\n"
        "v_add_f32_dpp v88, v88, v88 row_bcast:15 row_mask:0xa bank_mask:0xf\n"
        "v_add_f32_dpp v90, v90, v90 row_bcast:15 row_mask:0xa bank_mask:0xf\n"
        "v_mfma_f32_16x16x32_bf16 v[56:59], v[132:135], a[92:95], v[56:59]\n"
        "v_add_f32_dpp v85, v85, v85 row_bcast:15 row_mask:0xa bank_mask:0xf\n"
        "v_add_f32_dpp v87, v87, v87 row_bcast:15 row_mask:0xa bank_mask:0xf\n"
        "v_add_f32_dpp v89, v89, v89 row_bcast:15 row_mask:0xa bank_mask:0xf\n"
        "v_add_f32_dpp v91, v91, v91 row_bcast:15 row_mask:0xa bank_mask:0xf\n"
        "v_mfma_f32_16x16x32_bf16 v[52:55], v[136:139], a[40:43], v[52:55]\n"
        "v_add_f32_dpp v84, v84, v84 row_bcast:31 row_mask:0xc bank_mask:0xf\n"
        "v_add_f32_dpp v86, v86, v86 row_bcast:31 row_mask:0xc bank_mask:0xf\n"
        "v_add_f32_dpp v88, v88, v88 row_bcast:31 row_mask:0xc bank_mask:0xf\n"
        "v_add_f32_dpp v90, v90, v90 row_bcast:31 row_mask:0xc bank_mask:0xf\n"
        "v_mfma_f32_16x16x32_bf16 v[56:59], v[136:139], a[96:99], v[56:59]\n"
        "v_add_f32_dpp v85, v85, v85 row_bcast:31 row_mask:0xc bank_mask:0xf\n"
        "v_add_f32_dpp v87, v87, v87 row_bcast:31 row_mask:0xc bank_mask:0xf\n"
        "v_add_f32_dpp v89, v89, v89 row_bcast:31 row_mask:0xc bank_mask:0xf\n"
        "v_add_f32_dpp v91, v91, v91 row_bcast:31 row_mask:0xc bank_mask:0xf\n"
        "v_mfma_f32_16x16x32_bf16 v[52:55], v[140:143], a[44:47], v[52:55]\n"
        "v_fma_f32 v85, -v84, v84, v85\n"
        "v_fma_f32 v87, -v86, v86, v87\n"
        "v_fma_f32 v89, -v88, v88, v89\n"
        "v_fma_f32 v91, -v90, v90, v91\n"
        "v_mfma_f32_16x16x32_bf16 v[56:59], v[140:143], a[100:103], v[56:59]\n"
        "v_max_f32 v85, 0, v85\n"
        "v_max_f32 v87, 0, v87\n"
        "v_max_f32 v89, 0, v89\n"
        "v_max_f32 v91, 0, v91\n"
        "v_mfma_f32_16x16x32_bf16 v[52:55], v[144:147], a[48:51], v[52:55]\n"
        "v_add_f32 v85, 0x3727c5ac, v85\n"
        "v_add_f32 v87, 0x3727c5ac, v87\n"
        "v_add_f32 v89, 0x3727c5ac, v89\n"
        "v_add_f32 v91, 0x3727c5ac, v91\n"
        "v_mfma_f32_16x16x32_bf16 v[56:59], v[144:147], a[104:107], v[56:59]\n"
        "v_rsq_f32 v85, v85\n"
        "v_rsq_f32 v87, v87\n"
        "v_rsq_f32 v89, v89\n"
        "v_rsq_f32 v91, v91\n"
        "v_mfma_f32_16x16x32_bf16 v[52:55], v[148:151], a[52:55], v[52:55]\n"
        "v_mul_f32 v84, v84, v85\n"
        "v_mul_f32 v86, v86, v87\n"
        "v_mul_f32 v88, v88, v89\n"
        "v_mul_f32 v90, v90, v91\n"
        "v_mfma_f32_16x16x32_bf16 v[56:59], v[148:151], a[108:111], v[56:59]\n"
        "v_readlane_b32 s74, v85, 63\n"
        "v_readlane_b32 s75, v87, 63\n"
        "v_readlane_b32 s76, v89, 63\n"
        "v_readlane_b32 s77, v91, 63\n"
        "v_readlane_b32 s70, v84, 63\n"
        "v_readlane_b32 s71, v86, 63\n"
        "v_readlane_b32 s72, v88, 63\n"
        "v_readlane_b32 s73, v90, 63\n"
        "s_nop 1\n"
        "v_mov_b32 v16, v60\n"
        "v_mov_b32 v17, v64\n"
        "v_mul_f32 v28, s70, v16\n"
        "v_mul_f32 v29, s70, v17\n"
        "v_fma_f32 v28, v52, s74, -v28\n"
        "v_fma_f32 v29, v56, s74, -v29\n"
        "v_cvt_pk_bf16_f32 v68, v28, v29\n"
        "s_nop 1\n"
        "v_mov_b32_dpp v28, v68 quad_perm:[1,0,3,2] row_mask:0xf bank_mask:0xf\n"
        "v_perm_b32 v68, v28, v68, v20\n"
        "v_min_u32 v68, -2, v68\n"
        "s_add_u32 s52, s65, 0\n"
        "v_xor_b32 v72, s35, v68\n"
        "buffer_store_dword v68, v12, s[36:39], s52 offen sc1\n"
        "buffer_store_dword v72, v12, s[36:39], s52 offen offset:128 sc1\n"
        "v_mul_f32 v28, s71, v16\n"
        "v_mul_f32 v29, s71, v17\n"
        "v_fma_f32 v28, v53, s75, -v28\n"
        "v_fma_f32 v29, v57, s75, -v29\n"
        "v_cvt_pk_bf16_f32 v69, v28, v29\n"
        "s_nop 1\n"
        "v_mov_b32_dpp v28, v69 quad_perm:[1,0,3,2] row_mask:0xf bank_mask:0xf\n"
        "v_perm_b32 v69, v28, v69, v20\n"
        "v_min_u32 v69, -2, v69\n"
        "s_add_u32 s52, s65, 32768\n"
        "v_xor_b32 v73, s35, v69\n"
        "buffer_store_dword v69, v12, s[36:39], s52 offen sc1\n"
        "buffer_store_dword v73, v12, s[36:39], s52 offen offset:128 sc1\n"
        "v_mul_f32 v28, s72, v16\n"
        "v_mul_f32 v29, s72, v17\n"
        "v_fma_f32 v28, v54, s76, -v28\n"
        "v_fma_f32 v29, v58, s76, -v29\n"
        "v_cvt_pk_bf16_f32 v70, v28, v29\n"
        "s_nop 1\n"
        "v_mov_b32_dpp v28, v70 quad_perm:[1,0,3,2] row_mask:0xf bank_mask:0xf\n"
        "v_perm_b32 v70, v28, v70, v20\n"
        "v_min_u32 v70, -2, v70\n"
        "s_add_u32 s52, s65, 65536\n"
        "v_xor_b32 v74, s35, v70\n"
        "buffer_store_dword v70, v12, s[36:39], s52 offen sc1\n"
        "buffer_store_dword v74, v12, s[36:39], s52 offen offset:128 sc1\n"
        "v_mul_f32 v28, s73, v16\n"
        "v_mul_f32 v29, s73, v17\n"
        "v_fma_f32 v28, v55, s77, -v28\n"
        "v_fma_f32 v29, v59, s77, -v29\n"
        "v_cvt_pk_bf16_f32 v71, v28, v29\n"
        "s_nop 1\n"
        "v_mov_b32_dpp v28, v71 quad_perm:[1,0,3,2] row_mask:0xf bank_mask:0xf\n"
        "v_perm_b32 v71, v28, v71, v20\n"
        "v_min_u32 v71, -2, v71\n"
        "s_add_u32 s52, s65, 98304\n"
        "v_xor_b32 v75, s35, v71\n"
        "buffer_store_dword v71, v12, s[36:39], s52 offen sc1\n"
        "buffer_store_dword v75, v12, s[36:39], s52 offen offset:128 sc1\n"
        "s_waitcnt vmcnt(24)\n"
        "s_barrier\n"
        "s_add_u32 m0, s59, 131072\n"
        "s_add_u32 s52, s57, 172032\n"
        "buffer_load_dwordx4 v31, s[40:43], s52 offen lds\n"
        "s_add_u32 m0, s59, 132096\n"
        "s_add_u32 s52, s57, 173824\n"
        "buffer_load_dwordx4 v32, s[40:43], s52 offen lds\n"
        "s_add_u32 m0, s59, 133120\n"
        "s_add_u32 s52, s57, 175616\n"
        "buffer_load_dwordx4 v31, s[40:43], s52 offen lds\n"
        "s_add_u32 m0, s59, 134144\n"
        "s_add_u32 s52, s57, 177408\n"
        "buffer_load_dwordx4 v32, s[40:43], s52 offen lds\n"
        "s_add_u32 m0, s59, 147456\n"
        "s_add_u32 s52, s57, 200704\n"
        "buffer_load_dwordx4 v31, s[40:43], s52 offen lds\n"
        "s_add_u32 m0, s59, 148480\n"
        "s_add_u32 s52, s57, 202496\n"
        "buffer_load_dwordx4 v32, s[40:43], s52 offen lds\n"
        "s_add_u32 m0, s59, 149504\n"
        "s_add_u32 s52, s57, 204288\n"
        "buffer_load_dwordx4 v31, s[40:43], s52 offen lds\n"
        "s_add_u32 m0, s59, 150528\n"
        "s_add_u32 s52, s57, 206080\n"
        "buffer_load_dwordx4 v32, s[40:43], s52 offen lds\n"
        "s_mul_i32 s52, s49, 128\n"
        "s_add_u32 s52, s52, 0\n"
        "v_xor_b32 v39, s52, v38\n"
        "v_lshl_add_u32 v39, v92, 9, v39\n"
        "s_mov_b32 s52, 32768\n"
        "v_add3_u32 v39, v39, v93, s52\n"
        "ds_read_b64_tr_b16 a[112:113], v39 offset:0\n"
        "ds_read_b64_tr_b16 a[114:115], v39 offset:2048\n"
        "ds_read_b64_tr_b16 a[116:117], v39 offset:16384\n"
        "ds_read_b64_tr_b16 a[118:119], v39 offset:18432\n"
        "s_mul_i32 s52, s49, 128\n"
        "s_add_u32 s52, s52, 32\n"
        "v_xor_b32 v39, s52, v38\n"
        "v_lshl_add_u32 v39, v92, 9, v39\n"
        "s_mov_b32 s52, 32768\n"
        "v_add3_u32 v39, v39, v93, s52\n"
        "ds_read_b64_tr_b16 a[144:145], v39 offset:0\n"
        "ds_read_b64_tr_b16 a[146:147], v39 offset:2048\n"
        "ds_read_b64_tr_b16 a[148:149], v39 offset:16384\n"
        "ds_read_b64_tr_b16 a[150:151], v39 offset:18432\n"
        "s_mul_i32 s52, s49, 128\n"
        "s_add_u32 s52, s52, 64\n"
        "v_xor_b32 v39, s52, v38\n"
        "v_lshl_add_u32 v39, v92, 9, v39\n"
        "s_mov_b32 s52, 32768\n"
        "v_add3_u32 v39, v39, v93, s52\n"
        "ds_read_b64_tr_b16 a[176:177], v39 offset:0\n"
        "ds_read_b64_tr_b16 a[178:179], v39 offset:2048\n"
        "ds_read_b64_tr_b16 a[180:181], v39 offset:16384\n"
        "ds_read_b64_tr_b16 a[182:183], v39 offset:18432\n"
        "s_mul_i32 s52, s49, 128\n"
        "s_add_u32 s52, s52, 96\n"
        "v_xor_b32 v39, s52, v38\n"
        "v_lshl_add_u32 v39, v92, 9, v39\n"
        "s_mov_b32 s52, 32768\n"
        "v_add3_u32 v39, v39, v93, s52\n"
        "ds_read_b64_tr_b16 a[208:209], v39 offset:0\n"
        "ds_read_b64_tr_b16 a[210:211], v39 offset:2048\n"
        "ds_read_b64_tr_b16 a[212:213], v39 offset:16384\n"
        "ds_read_b64_tr_b16 a[214:215], v39 offset:18432\n"
        "s_waitcnt lgkmcnt(0)\n"
        "s_waitcnt vmcnt(24)\n"
        "s_barrier\n"
        "s_mov_b64 s[40:41], %3\n"
        "s_and_b32 s41, s41, 0xffff\n"
        "s_mov_b32 s42, 1605632\n"
        "s_mov_b64 s[44:45], %4\n"
        "s_and_b32 s45, s45, 0xffff\n"
        "s_mov_b32 s46, 1605632\n"
        "s_lshl_b32 s57, s82, 7\n"
        "s_mul_i32 s52, s49, 28672\n"
        "s_add_u32 s57, s57, s52\n"
        "s_lshl_b32 s59, s49, 10\n"
        "s_add_u32 m0, s59, 32768\n"
        "s_add_u32 s52, s57, 0\n"
        "buffer_load_dwordx4 v33, s[40:43], s52 offen lds\n"
        "s_add_u32 m0, s59, 36864\n"
        "s_add_u32 s52, s57, 0\n"
        "buffer_load_dwordx4 v33, s[44:47], s52 offen lds\n"
        "s_add_u32 m0, s59, 40960\n"
        "s_add_u32 s52, s57, 114688\n"
        "buffer_load_dwordx4 v33, s[40:43], s52 offen lds\n"
        "s_add_u32 m0, s59, 45056\n"
        "s_add_u32 s52, s57, 114688\n"
        "buffer_load_dwordx4 v33, s[44:47], s52 offen lds\n"
        "s_add_u32 m0, s59, 49152\n"
        "s_add_u32 s52, s57, 229376\n"
        "buffer_load_dwordx4 v33, s[40:43], s52 offen lds\n"
        "s_add_u32 m0, s59, 53248\n"
        "s_add_u32 s52, s57, 229376\n"
        "buffer_load_dwordx4 v33, s[44:47], s52 offen lds\n"
        "s_add_u32 m0, s59, 57344\n"
        "s_add_u32 s52, s57, 344064\n"
        "buffer_load_dwordx4 v33, s[40:43], s52 offen lds\n"
        "s_add_u32 m0, s59, 61440\n"
        "s_add_u32 s52, s57, 344064\n"
        "buffer_load_dwordx4 v33, s[44:47], s52 offen lds\n"
        "s_mul_i32 s52, s49, 128\n"
        "s_add_u32 s52, s52, 0\n"
        "v_xor_b32 v39, s52, v38\n"
        "v_lshl_add_u32 v39, v92, 9, v39\n"
        "s_mov_b32 s52, 65536\n"
        "v_add3_u32 v39, v39, v93, s52\n"
        "ds_read_b64_tr_b16 a[120:121], v39 offset:0\n"
        "ds_read_b64_tr_b16 a[122:123], v39 offset:2048\n"
        "ds_read_b64_tr_b16 a[124:125], v39 offset:16384\n"
        "ds_read_b64_tr_b16 a[126:127], v39 offset:18432\n"
        "s_mul_i32 s52, s49, 128\n"
        "s_add_u32 s52, s52, 32\n"
        "v_xor_b32 v39, s52, v38\n"
        "v_lshl_add_u32 v39, v92, 9, v39\n"
        "s_mov_b32 s52, 65536\n"
        "v_add3_u32 v39, v39, v93, s52\n"
        "ds_read_b64_tr_b16 a[152:153], v39 offset:0\n"
        "ds_read_b64_tr_b16 a[154:155], v39 offset:2048\n"
        "ds_read_b64_tr_b16 a[156:157], v39 offset:16384\n"
        "ds_read_b64_tr_b16 a[158:159], v39 offset:18432\n"
        "s_mul_i32 s52, s49, 128\n"
        "s_add_u32 s52, s52, 64\n"
        "v_xor_b32 v39, s52, v38\n"
        "v_lshl_add_u32 v39, v92, 9, v39\n"
        "s_mov_b32 s52, 65536\n"
        "v_add3_u32 v39, v39, v93, s52\n"
        "ds_read_b64_tr_b16 a[184:185], v39 offset:0\n"
        "ds_read_b64_tr_b16 a[186:187], v39 offset:2048\n"
        "ds_read_b64_tr_b16 a[188:189], v39 offset:16384\n"
        "ds_read_b64_tr_b16 a[190:191], v39 offset:18432\n"
        "s_mul_i32 s52, s49, 128\n"
        "s_add_u32 s52, s52, 96\n"
        "v_xor_b32 v39, s52, v38\n"
        "v_lshl_add_u32 v39, v92, 9, v39\n"
        "s_mov_b32 s52, 65536\n"
        "v_add3_u32 v39, v39, v93, s52\n"
        "ds_read_b64_tr_b16 a[216:217], v39 offset:0\n"
        "ds_read_b64_tr_b16 a[218:219], v39 offset:2048\n"
        "ds_read_b64_tr_b16 a[220:221], v39 offset:16384\n"
        "ds_read_b64_tr_b16 a[222:223], v39 offset:18432\n"
        "s_waitcnt lgkmcnt(0)\n"
        "s_waitcnt vmcnt(24)\n"
        "s_barrier\n"
        "s_add_u32 m0, s59, 65536\n"
        "s_add_u32 s52, s57, 458752\n"
        "buffer_load_dwordx4 v33, s[40:43], s52 offen lds\n"
        "s_add_u32 m0, s59, 69632\n"
        "s_add_u32 s52, s57, 458752\n"
        "buffer_load_dwordx4 v33, s[44:47], s52 offen lds\n"
        "s_add_u32 m0, s59, 73728\n"
        "s_add_u32 s52, s57, 573440\n"
        "buffer_load_dwordx4 v33, s[40:43], s52 offen lds\n"
        "s_add_u32 m0, s59, 77824\n"
        "s_add_u32 s52, s57, 573440\n"
        "buffer_load_dwordx4 v33, s[44:47], s52 offen lds\n"
        "s_add_u32 m0, s59, 81920\n"
        "s_add_u32 s52, s57, 688128\n"
        "buffer_load_dwordx4 v33, s[40:43], s52 offen lds\n"
        "s_add_u32 m0, s59, 86016\n"
        "s_add_u32 s52, s57, 688128\n"
        "buffer_load_dwordx4 v33, s[44:47], s52 offen lds\n"
        "s_add_u32 m0, s59, 90112\n"
        "s_add_u32 s52, s57, 802816\n"
        "buffer_load_dwordx4 v33, s[40:43], s52 offen lds\n"
        "s_add_u32 m0, s59, 94208\n"
        "s_add_u32 s52, s57, 802816\n"
        "buffer_load_dwordx4 v33, s[44:47], s52 offen lds\n"
        "s_mul_i32 s52, s49, 128\n"
        "s_add_u32 s52, s52, 0\n"
        "v_xor_b32 v39, s52, v38\n"
        "v_lshl_add_u32 v39, v92, 9, v39\n"
        "s_mov_b32 s52, 98304\n"
        "v_add3_u32 v39, v39, v93, s52\n"
        "ds_read_b64_tr_b16 a[128:129], v39 offset:0\n"
        "ds_read_b64_tr_b16 a[130:131], v39 offset:2048\n"
        "ds_read_b64_tr_b16 a[132:133], v39 offset:16384\n"
        "ds_read_b64_tr_b16 a[134:135], v39 offset:18432\n"
        "s_mul_i32 s52, s49, 128\n"
        "s_add_u32 s52, s52, 32\n"
        "v_xor_b32 v39, s52, v38\n"
        "v_lshl_add_u32 v39, v92, 9, v39\n"
        "s_mov_b32 s52, 98304\n"
        "v_add3_u32 v39, v39, v93, s52\n"
        "ds_read_b64_tr_b16 a[160:161], v39 offset:0\n"
        "ds_read_b64_tr_b16 a[162:163], v39 offset:2048\n"
        "ds_read_b64_tr_b16 a[164:165], v39 offset:16384\n"
        "ds_read_b64_tr_b16 a[166:167], v39 offset:18432\n"
        "s_mul_i32 s52, s49, 128\n"
        "s_add_u32 s52, s52, 64\n"
        "v_xor_b32 v39, s52, v38\n"
        "v_lshl_add_u32 v39, v92, 9, v39\n"
        "s_mov_b32 s52, 98304\n"
        "v_add3_u32 v39, v39, v93, s52\n"
        "ds_read_b64_tr_b16 a[192:193], v39 offset:0\n"
        "ds_read_b64_tr_b16 a[194:195], v39 offset:2048\n"
        "ds_read_b64_tr_b16 a[196:197], v39 offset:16384\n"
        "ds_read_b64_tr_b16 a[198:199], v39 offset:18432\n"
        "s_mul_i32 s52, s49, 128\n"
        "s_add_u32 s52, s52, 96\n"
        "v_xor_b32 v39, s52, v38\n"
        "v_lshl_add_u32 v39, v92, 9, v39\n"
        "s_mov_b32 s52, 98304\n"
        "v_add3_u32 v39, v39, v93, s52\n"
        "ds_read_b64_tr_b16 a[224:225], v39 offset:0\n"
        "ds_read_b64_tr_b16 a[226:227], v39 offset:2048\n"
        "ds_read_b64_tr_b16 a[228:229], v39 offset:16384\n"
        "ds_read_b64_tr_b16 a[230:231], v39 offset:18432\n"
        "s_waitcnt lgkmcnt(0)\n"
        "s_waitcnt vmcnt(16)\n"
        "s_barrier\n"
        "s_add_u32 m0, s59, 98304\n"
        "s_add_u32 s52, s57, 917504\n"
        "buffer_load_dwordx4 v33, s[40:43], s52 offen lds\n"
        "s_add_u32 m0, s59, 102400\n"
        "s_add_u32 s52, s57, 917504\n"
        "buffer_load_dwordx4 v33, s[44:47], s52 offen lds\n"
        "s_add_u32 m0, s59, 106496\n"
        "s_add_u32 s52, s57, 1032192\n"
        "buffer_load_dwordx4 v33, s[40:43], s52 offen lds\n"
        "s_add_u32 m0, s59, 110592\n"
        "s_add_u32 s52, s57, 1032192\n"
        "buffer_load_dwordx4 v33, s[44:47], s52 offen lds\n"
        "s_add_u32 m0, s59, 114688\n"
        "s_add_u32 s52, s57, 1146880\n"
        "buffer_load_dwordx4 v33, s[40:43], s52 offen lds\n"
        "s_add_u32 m0, s59, 118784\n"
        "s_add_u32 s52, s57, 1146880\n"
        "buffer_load_dwordx4 v33, s[44:47], s52 offen lds\n"
        "s_mul_i32 s52, s49, 128\n"
        "s_add_u32 s52, s52, 0\n"
        "v_xor_b32 v39, s52, v38\n"
        "v_lshl_add_u32 v39, v92, 9, v39\n"
        "s_mov_b32 s52, 131072\n"
        "v_add3_u32 v39, v39, v93, s52\n"
        "ds_read_b64_tr_b16 a[136:137], v39 offset:0\n"
        "ds_read_b64_tr_b16 a[138:139], v39 offset:2048\n"
        "ds_read_b64_tr_b16 a[140:141], v39 offset:16384\n"
        "ds_read_b64_tr_b16 a[142:143], v39 offset:18432\n"
        "s_mul_i32 s52, s49, 128\n"
        "s_add_u32 s52, s52, 32\n"
        "v_xor_b32 v39, s52, v38\n"
        "v_lshl_add_u32 v39, v92, 9, v39\n"
        "s_mov_b32 s52, 131072\n"
        "v_add3_u32 v39, v39, v93, s52\n"
        "ds_read_b64_tr_b16 a[168:169], v39 offset:0\n"
        "ds_read_b64_tr_b16 a[170:171], v39 offset:2048\n"
        "ds_read_b64_tr_b16 a[172:173], v39 offset:16384\n"
        "ds_read_b64_tr_b16 a[174:175], v39 offset:18432\n"
        "s_mul_i32 s52, s49, 128\n"
        "s_add_u32 s52, s52, 64\n"
        "v_xor_b32 v39, s52, v38\n"
        "v_lshl_add_u32 v39, v92, 9, v39\n"
        "s_mov_b32 s52, 131072\n"
        "v_add3_u32 v39, v39, v93, s52\n"
        "ds_read_b64_tr_b16 a[200:201], v39 offset:0\n"
        "ds_read_b64_tr_b16 a[202:203], v39 offset:2048\n"
        "ds_read_b64_tr_b16 a[204:205], v39 offset:16384\n"
        "ds_read_b64_tr_b16 a[206:207], v39 offset:18432\n"
        "s_mul_i32 s52, s49, 128\n"
        "s_add_u32 s52, s52, 96\n"
        "v_xor_b32 v39, s52, v38\n"
        "v_lshl_add_u32 v39, v92, 9, v39\n"
        "s_mov_b32 s52, 131072\n"
        "v_add3_u32 v39, v39, v93, s52\n"
        "ds_read_b64_tr_b16 a[232:233], v39 offset:0\n"
        "ds_read_b64_tr_b16 a[234:235], v39 offset:2048\n"
        "ds_read_b64_tr_b16 a[236:237], v39 offset:16384\n"
        "ds_read_b64_tr_b16 a[238:239], v39 offset:18432\n"
        "s_waitcnt lgkmcnt(0)\n"
        "s_waitcnt vmcnt(14)\n"
        "s_barrier\n"
        "s_add_u32 m0, s59, 131072\n"
        "s_add_u32 s52, s57, 1261568\n"
        "buffer_load_dwordx4 v33, s[40:43], s52 offen lds\n"
        "s_add_u32 m0, s59, 135168\n"
        "s_add_u32 s52, s57, 1261568\n"
        "buffer_load_dwordx4 v33, s[44:47], s52 offen lds\n"
        "s_add_u32 m0, s59, 139264\n"
        "s_add_u32 s52, s57, 1376256\n"
        "buffer_load_dwordx4 v33, s[40:43], s52 offen lds\n"
        "s_add_u32 m0, s59, 143360\n"
        "s_add_u32 s52, s57, 1376256\n"
        "buffer_load_dwordx4 v33, s[44:47], s52 offen lds\n"
        "s_add_u32 m0, s59, 147456\n"
        "s_add_u32 s52, s57, 1490944\n"
        "buffer_load_dwordx4 v33, s[40:43], s52 offen lds\n"
        "s_add_u32 m0, s59, 151552\n"
        "s_add_u32 s52, s57, 1490944\n"
        "buffer_load_dwordx4 v33, s[44:47], s52 offen lds\n"
        "v_lshrrev_b32 v39, 1, v38\n"
        "v_and_b32 v39, 0x60, v39\n"
        "s_lshl_b32 s52, s49, 5\n"
        "v_xor_b32 v39, s52, v39\n"
        "v_lshl_add_u32 v39, v92, 7, v39\n"
        "s_mov_b32 s52, 32768\n"
        "v_add3_u32 v39, v39, v93, s52\n"
        "ds_read_b64_tr_b16 v[96:97], v39 offset:0\n"
        "ds_read_b64_tr_b16 v[98:99], v39 offset:512\n"
        "ds_read_b64_tr_b16 v[104:105], v39 offset:8192\n"
        "ds_read_b64_tr_b16 v[106:107], v39 offset:8704\n"
        "ds_read_b64_tr_b16 v[112:113], v39 offset:16384\n"
        "ds_read_b64_tr_b16 v[114:115], v39 offset:16896\n"
        "ds_read_b64_tr_b16 v[120:121], v39 offset:24576\n"
        "ds_read_b64_tr_b16 v[122:123], v39 offset:25088\n"
        "ds_read_b64_tr_b16 v[100:101], v39 offset:4096\n"
        "ds_read_b64_tr_b16 v[102:103], v39 offset:4608\n"
        "ds_read_b64_tr_b16 v[108:109], v39 offset:12288\n"
        "ds_read_b64_tr_b16 v[110:111], v39 offset:12800\n"
        "ds_read_b64_tr_b16 v[116:117], v39 offset:20480\n"
        "ds_read_b64_tr_b16 v[118:119], v39 offset:20992\n"
        "ds_read_b64_tr_b16 v[124:125], v39 offset:28672\n"
        "ds_read_b64_tr_b16 v[126:127], v39 offset:29184\n"
        "s_waitcnt lgkmcnt(0)\n"
        "v_mfma_f32_16x16x32_bf16 v[60:63], v[48:51], v[96:99], 0\n"
        "v_mfma_f32_16x16x32_bf16 v[64:67], v[48:51], v[100:103], 0\n"
        "v_mfma_f32_16x16x32_bf16 v[60:63], v[48:51], v[104:107], v[60:63]\n"
        "v_mfma_f32_16x16x32_bf16 v[64:67], v[48:51], v[108:111], v[64:67]\n"
        "v_mfma_f32_16x16x32_bf16 v[60:63], v[48:51], v[112:115], v[60:63]\n"
        "v_mfma_f32_16x16x32_bf16 v[64:67], v[48:51], v[116:119], v[64:67]\n"
        "v_mfma_f32_16x16x32_bf16 v[60:63], v[48:51], v[120:123], v[60:63]\n"
        "v_mfma_f32_16x16x32_bf16 v[64:67], v[48:51], v[124:127], v[64:67]\n"
        "s_waitcnt vmcnt(12)\n"
        "s_barrier\n"
        "s_mov_b64 s[40:41], %5\n"
        "s_and_b32 s41, s41, 0xffff\n"
        "s_mov_b32 s42, 1605632\n"
        "s_lshl_b32 s57, s82, 3\n"
        "s_add_u32 s57, s57, s49\n"
        "s_mul_i32 s57, s57, 7168\n"
        "s_lshl_b32 s59, s49, 13\n"
        "s_add_u32 m0, s59, 32768\n"
        "s_add_u32 s52, s57, 0\n"
        "buffer_load_dwordx4 v34, s[40:43], s52 offen lds\n"
        "s_add_u32 m0, s59, 33792\n"
        "s_add_u32 s52, s57, 896\n"
        "buffer_load_dwordx4 v35, s[40:43], s52 offen lds\n"
        "s_add_u32 m0, s59, 34816\n"
        "s_add_u32 s52, s57, 1792\n"
        "buffer_load_dwordx4 v36, s[40:43], s52 offen lds\n"
        "s_add_u32 m0, s59, 35840\n"
        "s_add_u32 s52, s57, 2688\n"
        "buffer_load_dwordx4 v37, s[40:43], s52 offen lds\n"
        "s_add_u32 m0, s59, 36864\n"
        "s_add_u32 s52, s57, 3584\n"
        "buffer_load_dwordx4 v34, s[40:43], s52 offen lds\n"
        "s_add_u32 m0, s59, 37888\n"
        "s_add_u32 s52, s57, 4480\n"
        "buffer_load_dwordx4 v35, s[40:43], s52 offen lds\n"
        "s_add_u32 m0, s59, 38912\n"
        "s_add_u32 s52, s57, 5376\n"
        "buffer_load_dwordx4 v36, s[40:43], s52 offen lds\n"
        "s_add_u32 m0, s59, 39936\n"
        "s_add_u32 s52, s57, 6272\n"
        "buffer_load_dwordx4 v37, s[40:43], s52 offen lds\n"
        "v_lshrrev_b32 v39, 1, v38\n"
        "v_and_b32 v39, 0x60, v39\n"
        "s_lshl_b32 s52, s49, 5\n"
        "v_xor_b32 v39, s52, v39\n"
        "v_lshl_add_u32 v39, v92, 7, v39\n"
        "s_mov_b32 s52, 65536\n"
        "v_add3_u32 v39, v39, v93, s52\n"
        "ds_read_b64_tr_b16 v[128:129], v39 offset:0\n"
        "ds_read_b64_tr_b16 v[130:131], v39 offset:512\n"
        "ds_read_b64_tr_b16 v[136:137], v39 offset:8192\n"
        "ds_read_b64_tr_b16 v[138:139], v39 offset:8704\n"
        "ds_read_b64_tr_b16 v[144:145], v39 offset:16384\n"
        "ds_read_b64_tr_b16 v[146:147], v39 offset:16896\n"
        "ds_read_b64_tr_b16 v[152:153], v39 offset:24576\n"
        "ds_read_b64_tr_b16 v[154:155], v39 offset:25088\n"
        "ds_read_b64_tr_b16 v[132:133], v39 offset:4096\n"
        "ds_read_b64_tr_b16 v[134:135], v39 offset:4608\n"
        "ds_read_b64_tr_b16 v[140:141], v39 offset:12288\n"
        "ds_read_b64_tr_b16 v[142:143], v39 offset:12800\n"
        "ds_read_b64_tr_b16 v[148:149], v39 offset:20480\n"
        "ds_read_b64_tr_b16 v[150:151], v39 offset:20992\n"
        "ds_read_b64_tr_b16 v[156:157], v39 offset:28672\n"
        "ds_read_b64_tr_b16 v[158:159], v39 offset:29184\n"
        "s_waitcnt lgkmcnt(0)\n"
        "v_mfma_f32_16x16x32_bf16 v[60:63], v[48:51], v[128:131], v[60:63]\n"
        "v_mfma_f32_16x16x32_bf16 v[64:67], v[48:51], v[132:135], v[64:67]\n"
        "v_mfma_f32_16x16x32_bf16 v[60:63], v[48:51], v[136:139], v[60:63]\n"
        "v_mfma_f32_16x16x32_bf16 v[64:67], v[48:51], v[140:143], v[64:67]\n"
        "v_mfma_f32_16x16x32_bf16 v[60:63], v[48:51], v[144:147], v[60:63]\n"
        "v_mfma_f32_16x16x32_bf16 v[64:67], v[48:51], v[148:151], v[64:67]\n"
        "v_mfma_f32_16x16x32_bf16 v[60:63], v[48:51], v[152:155], v[60:63]\n"
        "v_mfma_f32_16x16x32_bf16 v[64:67], v[48:51], v[156:159], v[64:67]\n"
        "s_waitcnt vmcnt(14)\n"
        "s_barrier\n"
        "s_add_u32 m0, s59, 65536\n"
        "s_add_u32 s52, s57, 28672\n"
        "buffer_load_dwordx4 v34, s[40:43], s52 offen lds\n"
        "s_add_u32 m0, s59, 66560\n"
        "s_add_u32 s52, s57, 29568\n"
        "buffer_load_dwordx4 v35, s[40:43], s52 offen lds\n"
        "s_add_u32 m0, s59, 67584\n"
        "s_add_u32 s52, s57, 30464\n"
        "buffer_load_dwordx4 v36, s[40:43], s52 offen lds\n"
        "s_add_u32 m0, s59, 68608\n"
        "s_add_u32 s52, s57, 31360\n"
        "buffer_load_dwordx4 v37, s[40:43], s52 offen lds\n"
        "s_add_u32 m0, s59, 69632\n"
        "s_add_u32 s52, s57, 32256\n"
        "buffer_load_dwordx4 v34, s[40:43], s52 offen lds\n"
        "s_add_u32 m0, s59, 70656\n"
        "s_add_u32 s52, s57, 33152\n"
        "buffer_load_dwordx4 v35, s[40:43], s52 offen lds\n"
        "s_add_u32 m0, s59, 71680\n"
        "s_add_u32 s52, s57, 34048\n"
        "buffer_load_dwordx4 v36, s[40:43], s52 offen lds\n"
        "s_add_u32 m0, s59, 72704\n"
        "s_add_u32 s52, s57, 34944\n"
        "buffer_load_dwordx4 v37, s[40:43], s52 offen lds\n"
        "v_lshrrev_b32 v39, 1, v38\n"
        "v_and_b32 v39, 0x60, v39\n"
        "s_lshl_b32 s52, s49, 5\n"
        "v_xor_b32 v39, s52, v39\n"
        "v_lshl_add_u32 v39, v92, 7, v39\n"
        "s_mov_b32 s52, 98304\n"
        "v_add3_u32 v39, v39, v93, s52\n"
        "ds_read_b64_tr_b16 v[160:161], v39 offset:0\n"
        "ds_read_b64_tr_b16 v[162:163], v39 offset:512\n"
        "ds_read_b64_tr_b16 v[168:169], v39 offset:8192\n"
        "ds_read_b64_tr_b16 v[170:171], v39 offset:8704\n"
        "ds_read_b64_tr_b16 v[176:177], v39 offset:16384\n"
        "ds_read_b64_tr_b16 v[178:179], v39 offset:16896\n"
        "ds_read_b64_tr_b16 v[164:165], v39 offset:4096\n"
        "ds_read_b64_tr_b16 v[166:167], v39 offset:4608\n"
        "ds_read_b64_tr_b16 v[172:173], v39 offset:12288\n"
        "ds_read_b64_tr_b16 v[174:175], v39 offset:12800\n"
        "ds_read_b64_tr_b16 v[180:181], v39 offset:20480\n"
        "ds_read_b64_tr_b16 v[182:183], v39 offset:20992\n"
        "s_waitcnt lgkmcnt(0)\n"
        "v_mfma_f32_16x16x32_bf16 v[60:63], v[48:51], v[160:163], v[60:63]\n"
        "v_mfma_f32_16x16x32_bf16 v[64:67], v[48:51], v[164:167], v[64:67]\n"
        "v_mfma_f32_16x16x32_bf16 v[60:63], v[48:51], v[168:171], v[60:63]\n"
        "v_mfma_f32_16x16x32_bf16 v[64:67], v[48:51], v[172:175], v[64:67]\n"
        "v_mfma_f32_16x16x32_bf16 v[60:63], v[48:51], v[176:179], v[60:63]\n"
        "v_mfma_f32_16x16x32_bf16 v[64:67], v[48:51], v[180:183], v[64:67]\n"
        "s_waitcnt vmcnt(16)\n"
        "s_barrier\n"
        "v_lshrrev_b32 v39, 1, v38\n"
        "v_and_b32 v39, 0x60, v39\n"
        "s_lshl_b32 s52, s49, 5\n"
        "v_xor_b32 v39, s52, v39\n"
        "v_lshl_add_u32 v39, v92, 7, v39\n"
        "s_mov_b32 s52, 131072\n"
        "v_add3_u32 v39, v39, v93, s52\n"
        "ds_read_b64_tr_b16 v[184:185], v39 offset:0\n"
        "ds_read_b64_tr_b16 v[186:187], v39 offset:512\n"
        "ds_read_b64_tr_b16 v[192:193], v39 offset:8192\n"
        "ds_read_b64_tr_b16 v[194:195], v39 offset:8704\n"
        "ds_read_b64_tr_b16 v[200:201], v39 offset:16384\n"
        "ds_read_b64_tr_b16 v[202:203], v39 offset:16896\n"
        "ds_read_b64_tr_b16 v[188:189], v39 offset:4096\n"
        "ds_read_b64_tr_b16 v[190:191], v39 offset:4608\n"
        "ds_read_b64_tr_b16 v[196:197], v39 offset:12288\n"
        "ds_read_b64_tr_b16 v[198:199], v39 offset:12800\n"
        "ds_read_b64_tr_b16 v[204:205], v39 offset:20480\n"
        "ds_read_b64_tr_b16 v[206:207], v39 offset:20992\n"
        "s_waitcnt lgkmcnt(0)\n"
        "v_mfma_f32_16x16x32_bf16 v[60:63], v[48:51], v[184:187], v[60:63]\n"
        "v_mfma_f32_16x16x32_bf16 v[64:67], v[48:51], v[188:191], v[64:67]\n"
        "v_mfma_f32_16x16x32_bf16 v[60:63], v[48:51], v[192:195], v[60:63]\n"
        "v_mfma_f32_16x16x32_bf16 v[64:67], v[48:51], v[196:199], v[64:67]\n"
        "v_mfma_f32_16x16x32_bf16 v[60:63], v[48:51], v[200:203], v[60:63]\n"
        "v_mfma_f32_16x16x32_bf16 v[64:67], v[48:51], v[204:207], v[64:67]\n"
        "s_waitcnt vmcnt(8)\n"
        "s_barrier\n"
        "s_mul_i32 s52, s49, 224\n"
        "s_add_u32 s52, s52, 0\n"
        "v_xor_b32 v39, s52, v38\n"
        "v_lshl_add_u32 v39, v92, 10, v39\n"
        "s_mov_b32 s52, 32768\n"
        "v_add3_u32 v39, v39, v93, s52\n"
        "ds_read_b64_tr_b16 a[240:241], v39 offset:0\n"
        "ds_read_b64_tr_b16 a[242:243], v39 offset:4096\n"
        "s_mul_i32 s52, s49, 224\n"
        "s_add_u32 s52, s52, 32\n"
        "v_xor_b32 v39, s52, v38\n"
        "v_lshl_add_u32 v39, v92, 10, v39\n"
        "s_mov_b32 s52, 32768\n"
        "v_add3_u32 v39, v39, v93, s52\n"
        "ds_read_b64_tr_b16 a[244:245], v39 offset:0\n"
        "ds_read_b64_tr_b16 a[246:247], v39 offset:4096\n"
        "s_mul_i32 s52, s49, 224\n"
        "s_add_u32 s52, s52, 64\n"
        "v_xor_b32 v39, s52, v38\n"
        "v_lshl_add_u32 v39, v92, 10, v39\n"
        "s_mov_b32 s52, 32768\n"
        "v_add3_u32 v39, v39, v93, s52\n"
        "ds_read_b64_tr_b16 a[252:253], v39 offset:0\n"
        "ds_read_b64_tr_b16 a[254:255], v39 offset:4096\n"
        "s_mul_i32 s52, s49, 224\n"
        "s_add_u32 s52, s52, 96\n"
        "v_xor_b32 v39, s52, v38\n"
        "v_lshl_add_u32 v39, v92, 10, v39\n"
        "s_mov_b32 s52, 32768\n"
        "v_add3_u32 v39, v39, v93, s52\n"
        "ds_read_b64_tr_b16 v[240:241], v39 offset:0\n"
        "ds_read_b64_tr_b16 v[242:243], v39 offset:4096\n"
        "s_mul_i32 s52, s49, 224\n"
        "s_add_u32 s52, s52, 128\n"
        "v_xor_b32 v39, s52, v38\n"
        "v_lshl_add_u32 v39, v92, 10, v39\n"
        "s_mov_b32 s52, 32768\n"
        "v_add3_u32 v39, v39, v93, s52\n"
        "ds_read_b64_tr_b16 v[244:245], v39 offset:0\n"
        "ds_read_b64_tr_b16 v[246:247], v39 offset:4096\n"
        "s_mul_i32 s52, s49, 224\n"
        "s_add_u32 s52, s52, 160\n"
        "v_xor_b32 v39, s52, v38\n"
        "v_lshl_add_u32 v39, v92, 10, v39\n"
        "s_mov_b32 s52, 32768\n"
        "v_add3_u32 v39, v39, v93, s52\n"
        "ds_read_b64_tr_b16 v[248:249], v39 offset:0\n"
        "ds_read_b64_tr_b16 v[250:251], v39 offset:4096\n"
        "s_mul_i32 s52, s49, 224\n"
        "s_add_u32 s52, s52, 192\n"
        "v_xor_b32 v39, s52, v38\n"
        "v_lshl_add_u32 v39, v92, 10, v39\n"
        "s_mov_b32 s52, 32768\n"
        "v_add3_u32 v39, v39, v93, s52\n"
        "ds_read_b64_tr_b16 v[252:253], v39 offset:0\n"
        "ds_read_b64_tr_b16 v[254:255], v39 offset:4096\n"
        "s_waitcnt lgkmcnt(0)\n"
        "s_waitcnt vmcnt(0)\n"
        "s_barrier\n"
        "v_mov_b32 v18, v60\n"
        "v_mov_b32 v19, v64\n"
        "s_mul_i32 s52, s49, 224\n"
        "s_add_u32 s52, s52, 0\n"
        "v_xor_b32 v39, s52, v38\n"
        "v_lshl_add_u32 v39, v92, 10, v39\n"
        "s_mov_b32 s52, 65536\n"
        "v_add3_u32 v39, v39, v93, s52\n"
        "ds_read_b64_tr_b16 v[40:41], v39 offset:0\n"
        "ds_read_b64_tr_b16 v[42:43], v39 offset:4096\n"
        "s_mul_i32 s52, s49, 224\n"
        "s_add_u32 s52, s52, 32\n"
        "v_xor_b32 v39, s52, v38\n"
        "v_lshl_add_u32 v39, v92, 10, v39\n"
        "s_mov_b32 s52, 65536\n"
        "v_add3_u32 v39, v39, v93, s52\n"
        "ds_read_b64_tr_b16 v[44:45], v39 offset:0\n"
        "ds_read_b64_tr_b16 v[46:47], v39 offset:4096\n"
        "s_mul_i32 s52, s49, 224\n"
        "s_add_u32 s52, s52, 64\n"
        "v_xor_b32 v39, s52, v38\n"
        "v_lshl_add_u32 v39, v92, 10, v39\n"
        "s_mov_b32 s52, 65536\n"
        "v_add3_u32 v39, v39, v93, s52\n"
        "ds_read_b64_tr_b16 v[48:49], v39 offset:0\n"
        "ds_read_b64_tr_b16 v[50:51], v39 offset:4096\n"
        "s_mul_i32 s52, s49, 224\n"
        "s_add_u32 s52, s52, 96\n"
        "v_xor_b32 v39, s52, v38\n"
        "v_lshl_add_u32 v39, v92, 10, v39\n"
        "s_mov_b32 s52, 65536\n"
        "v_add3_u32 v39, v39, v93, s52\n"
        "ds_read_b64_tr_b16 v[52:53], v39 offset:0\n"
        "ds_read_b64_tr_b16 v[54:55], v39 offset:4096\n"
        "s_mul_i32 s52, s49, 224\n"
        "s_add_u32 s52, s52, 128\n"
        "v_xor_b32 v39, s52, v38\n"
        "v_lshl_add_u32 v39, v92, 10, v39\n"
        "s_mov_b32 s52, 65536\n"
        "v_add3_u32 v39, v39, v93, s52\n"
        "ds_read_b64_tr_b16 v[56:57], v39 offset:0\n"
        "ds_read_b64_tr_b16 v[58:59], v39 offset:4096\n"
        "s_mul_i32 s52, s49, 224\n"
        "s_add_u32 s52, s52, 160\n"
        "v_xor_b32 v39, s52, v38\n"
        "v_lshl_add_u32 v39, v92, 10, v39\n"
        "s_mov_b32 s52, 65536\n"
        "v_add3_u32 v39, v39, v93, s52\n"
        "ds_read_b64_tr_b16 v[60:61], v39 offset:0\n"
        "ds_read_b64_tr_b16 v[62:63], v39 offset:4096\n"
        "s_mul_i32 s52, s49, 224\n"
        "s_add_u32 s52, s52, 192\n"
        "v_xor_b32 v39, s52, v38\n"
        "v_lshl_add_u32 v39, v92, 10, v39\n"
        "s_mov_b32 s52, 65536\n"
        "v_add3_u32 v39, v39, v93, s52\n"
        "ds_read_b64_tr_b16 v[64:65], v39 offset:0\n"
        "ds_read_b64_tr_b16 v[66:67], v39 offset:4096\n"
        "s_waitcnt lgkmcnt(0)\n"
        "ds_write_b128 v218, v[40:43] offset:0\n"
        "ds_write_b128 v218, v[44:47] offset:1024\n"
        "ds_write_b128 v218, v[48:51] offset:2048\n"
        "ds_write_b128 v218, v[52:55] offset:3072\n"
        "ds_write_b128 v218, v[56:59] offset:4096\n"
        "ds_write_b128 v218, v[60:63] offset:5120\n"
        "ds_write_b128 v218, v[64:67] offset:6144\n"
        "s_waitcnt lgkmcnt(0)\n"
        "s_mov_b64 s[40:41], %6\n"
        "s_and_b32 s41, s41, 0xffff\n"
        "s_mov_b32 s42, 3584\n"
        "buffer_load_dword v208, off, s[40:43], 0\n"
        "s_lshr_b32 s52, s48, 1\n"
        "s_mul_i32 s52, s52, 14\n"
        "v_min_u32 v27, 45, v24\n"
        "v_cmp_gt_u32 vcc, 32, v27\n"
        "v_add_u32 v28, s52, v27\n"
        "s_mov_b32 s52, 4096\n"
        "v_cndmask_b32 v27, v28, v27, vcc\n"
        "v_lshlrev_b32 v27, 7, v27\n"
        ".Lch_rdy_%=:\n"
        "buffer_load_dwordx2 v[28:29], v27, s[36:39], s52 offen sc1\n"
        "s_waitcnt vmcnt(0)\n"
        "v_not_b32 v29, v29\n"
        "v_xor_b32 v28, s35, v28\n"
        "v_xor_b32 v29, s35, v29\n"
        "v_or_b32 v28, v28, v29\n"
        "v_cmp_ne_u32 vcc, 0, v28\n"
        "s_cbranch_vccz .Lch_rdy_ok_%=\n"
        "s_sleep 2\n"
        "s_add_u32 s55, s55, 1\n"
        "s_cmp_lt_u32 s55, 200000\n"
        "s_cbranch_scc1 .Lch_rdy_%=\n"
        ".Lch_rdy_ok_%=:\n"
        "s_add_u32 s97, s68, 1238\n"
        "s_mov_b32 s68, 0x35452410\n"
        "s_mov_b32 s69, 0x76\n"
        "s_mov_b32 s50, 0\n"
        "s_mov_b32 s83, 4096\n"
        ".Lch_op_%=:\n"
        "s_and_b32 s51, s68, 7\n"
        "s_lshr_b64 s[68:69], s[68:69], 4\n"
        "s_and_b32 s56, s51, 3\n"
        "s_mul_i32 s56, s56, 896\n"
        "v_add_u32 v22, s56, v4\n"
        "s_bitcmp1_b32 0x39f, s50\n"
        "s_cbranch_scc0 .Lch_no1_%=\n"
        "s_mul_i32 s57, s51, 16384\n"
        "s_add_u32 s57, s57, s61\n"
        "s_xor_b32 s83, s83, 512\n"
        "v_add_u32 v30, s83, v3\n"
        "v_add_u32 v31, s83, v4\n"
        "v_mov_b32 v27, 0xff800000\n"
        "s_bitcmp1_b32 0x114, s50\n"
        "s_cbranch_scc0 .Lch_p0_%=\n"
        "s_waitcnt vmcnt(1)\n"
        "s_branch .Lch_p1_%=\n"
        ".Lch_p0_%=:\n"
        "s_mul_i32 s91, s51, 143360\n"
        "s_add_u32 s91, s91, s60\n"
        "s_add_u32 s92, s91, 5120\n"
        "s_bitcmp1_b32 0xb, s50\n"
        "s_cbranch_scc0 .Lch_p_g_%=\n"
        ".Lch_p_w_%=:\n"
        "s_memrealtime s[78:79]\n"
        "s_waitcnt lgkmcnt(0)\n"
        "s_sub_u32 s78, s78, s97\n"
        "s_cmp_lt_i32 s78, 100\n"
        "s_cbranch_scc1 .Lch_p_w_%=\n"
        ".Lch_p_g_%=:\n"
        "s_bitcmp1_b32 0x7f, s50\n"
        "s_cbranch_scc0 .Lch_pf_ok_%=\n"
        ".Lch_pf_%=:\n"
        "buffer_load_dwordx2 v[216:217], v0, s[36:39], s91 offen sc1\n"
        "s_waitcnt vmcnt(0)\n"
        "v_cmp_eq_u32 vcc, -1, v217\n"
        "s_cbranch_vccz .Lch_pf_done_%=\n"
        "s_bitcmp1_b32 0x7f, s50\n"
        "s_cbranch_scc0 .Lch_pf_ns_%=\n"
        "s_sleep 8\n"
        ".Lch_pf_ns_%=:\n"
        "s_add_u32 s55, s55, 1\n"
        "s_cmp_lt_u32 s55, 200000\n"
        "s_cbranch_scc1 .Lch_pf_%=\n"
        ".Lch_pf_done_%=:\n"
        "buffer_load_dwordx4 v[208:211], v1, s[36:39], s91 offen sc1\n"
        "buffer_load_dwordx4 v[212:215], v2, s[36:39], s92 offen sc1\n"
        "s_waitcnt vmcnt(0)\n"
        "s_branch .Lch_p1_%=\n"
        ".Lch_pf_ok_%=:\n"
        ".Lch_p_%=:\n"
        "buffer_load_dwordx2 v[216:217], v0, s[36:39], s91 offen sc1\n"
        "buffer_load_dwordx4 v[208:211], v1, s[36:39], s91 offen sc1\n"
        "buffer_load_dwordx4 v[212:215], v2, s[36:39], s92 offen sc1\n"
        "s_waitcnt vmcnt(0)\n"
        ".Lch_p1_%=:\n"
        "v_cndmask_b32 v24, v27, v216, s[66:67]\n"
        "v_max3_u32 v28, v216, v217, v208\n"
        "v_max3_u32 v29, v209, v210, v211\n"
        "v_max_f32_dpp v26, v24, v24 row_ror:4 row_mask:0xf bank_mask:0xf\n"
        "v_max3_u32 v28, v28, v212, v213\n"
        "v_max3_u32 v29, v29, v214, v215\n"
        "v_max_f32_dpp v26, v26, v26 row_ror:8 row_mask:0xf bank_mask:0xf\n"
        "v_max_u32 v28, v28, v29\n"
        "v_mov_b32 v25, v26\n"
        "v_cmp_eq_u32 vcc, -1, v28\n"
        "s_cbranch_vccz .Lch_p_ok_%=\n"
        "s_bitcmp1_b32 0x7f, s50\n"
        "s_cbranch_scc0 .Lch_p_ns_%=\n"
        "s_sleep 8\n"
        ".Lch_p_ns_%=:\n"
        "s_add_u32 s55, s55, 1\n"
        "s_cmp_lt_u32 s55, 200000\n"
        "s_cbranch_scc1 .Lch_p_%=\n"
        ".Lch_p_ok_%=:\n"
        "v_permlane16_swap_b32 v26, v25\n"
        "v_max_f32 v26, v26, v25\n"
        "v_mov_b32 v25, v26\n"
        "s_nop 1\n"
        "v_permlane32_swap_b32 v26, v25\n"
        "v_max_f32 v26, v26, v25\n"
        "v_sub_f32 v28, v24, v26\n"
        "v_exp_f32 v28, v28\n"
        "s_nop 0\n"
        "v_mul_f32 v29, v28, v217\n"
        "v_cvt_pk_bf16_f32 v36, v28, v28\n"
        "v_cvt_pk_bf16_f32 v28, v28, v28\n"
        "v_add_f32_dpp v29, v29, v29 row_ror:4 row_mask:0xf bank_mask:0xf\n"
        "v_mov_b32_dpp v36, v36 row_shr:4 row_mask:0xf bank_mask:0xa\n"
        "v_mov_b32_dpp v28, v28 row_shl:4 row_mask:0xf bank_mask:0x5\n"
        "v_add_f32_dpp v29, v29, v29 row_ror:8 row_mask:0xf bank_mask:0xf\n"
        "v_and_b32 v40, v36, v220\n"
        "v_and_b32 v41, v36, v221\n"
        "v_and_b32 v42, v36, v222\n"
        "v_and_b32 v43, v36, v223\n"
        "v_and_b32 v44, v28, v220\n"
        "v_and_b32 v45, v28, v221\n"
        "v_and_b32 v46, v28, v222\n"
        "v_and_b32 v47, v28, v223\n"
        "v_mfma_f32_16x16x32_bf16 v[32:35], v[40:43], v[208:211], 0\n"
        "v_mov_b32 v25, v29\n"
        "v_mfma_f32_16x16x32_bf16 v[32:35], v[44:47], v[212:215], v[32:35]\n"
        "s_nop 0\n"
        "v_permlane16_swap_b32 v29, v25\n"
        "v_add_f32 v29, v29, v25\n"
        "v_mov_b32 v25, v29\n"
        "s_nop 1\n"
        "v_permlane32_swap_b32 v29, v25\n"
        "v_add_f32 v29, v29, v25\n"
        "v_permlane32_swap_b32 v32, v34\n"
        "v_permlane32_swap_b32 v33, v35\n"
        "v_rcp_f32 v29, v29\n"
        "v_add_f32_dpp v32, v34, v32 row_ror:8 row_mask:0xf bank_mask:0xf\n"
        "v_add_f32_dpp v33, v35, v33 row_ror:8 row_mask:0xf bank_mask:0xf\n"
        "v_mul_f32 v32, v32, v29\n"
        "v_mul_f32 v33, v33, v29\n"
        "v_cvt_pk_bf16_f32 v32, v32, v33\n"
        "ds_write_b32 v30, v32\n"
        "s_waitcnt lgkmcnt(0)\n"
        "s_barrier\n"
        "ds_read_b128 v[60:63], v31 offset:0\n"
        "ds_read_b128 v[64:67], v31 offset:64\n"
        "ds_read_b128 v[68:71], v31 offset:128\n"
        "ds_read_b128 v[72:75], v31 offset:192\n"
        "ds_read_b128 v[76:79], v31 offset:256\n"
        "ds_read_b128 v[80:83], v31 offset:320\n"
        "ds_read_b128 v[84:87], v31 offset:384\n"
        "ds_read_b128 v[88:91], v31 offset:448\n"
        "s_waitcnt lgkmcnt(7)\n"
        "v_mfma_f32_16x16x32_bf16 v[24:27], v[60:63], a[112:115], 0\n"
        "v_mfma_f32_16x16x32_bf16 v[28:31], v[60:63], a[144:147], 0\n"
        "v_mfma_f32_16x16x32_bf16 v[32:35], v[60:63], a[176:179], 0\n"
        "v_mfma_f32_16x16x32_bf16 v[36:39], v[60:63], a[208:211], 0\n"
        "s_waitcnt lgkmcnt(6)\n"
        "v_mfma_f32_16x16x32_bf16 v[24:27], v[64:67], a[116:119], v[24:27]\n"
        "v_mfma_f32_16x16x32_bf16 v[28:31], v[64:67], a[148:151], v[28:31]\n"
        "v_mfma_f32_16x16x32_bf16 v[32:35], v[64:67], a[180:183], v[32:35]\n"
        "v_mfma_f32_16x16x32_bf16 v[36:39], v[64:67], a[212:215], v[36:39]\n"
        "s_waitcnt lgkmcnt(5)\n"
        "v_mfma_f32_16x16x32_bf16 v[24:27], v[68:71], a[120:123], v[24:27]\n"
        "v_mfma_f32_16x16x32_bf16 v[28:31], v[68:71], a[152:155], v[28:31]\n"
        "v_mfma_f32_16x16x32_bf16 v[32:35], v[68:71], a[184:187], v[32:35]\n"
        "v_mfma_f32_16x16x32_bf16 v[36:39], v[68:71], a[216:219], v[36:39]\n"
        "s_waitcnt lgkmcnt(4)\n"
        "v_mfma_f32_16x16x32_bf16 v[24:27], v[72:75], a[124:127], v[24:27]\n"
        "v_mfma_f32_16x16x32_bf16 v[28:31], v[72:75], a[156:159], v[28:31]\n"
        "v_mfma_f32_16x16x32_bf16 v[32:35], v[72:75], a[188:191], v[32:35]\n"
        "v_mfma_f32_16x16x32_bf16 v[36:39], v[72:75], a[220:223], v[36:39]\n"
        "s_waitcnt lgkmcnt(3)\n"
        "v_mfma_f32_16x16x32_bf16 v[24:27], v[76:79], a[128:131], v[24:27]\n"
        "v_mfma_f32_16x16x32_bf16 v[28:31], v[76:79], a[160:163], v[28:31]\n"
        "v_mfma_f32_16x16x32_bf16 v[32:35], v[76:79], a[192:195], v[32:35]\n"
        "v_mfma_f32_16x16x32_bf16 v[36:39], v[76:79], a[224:227], v[36:39]\n"
        "s_waitcnt lgkmcnt(2)\n"
        "v_mfma_f32_16x16x32_bf16 v[24:27], v[80:83], a[132:135], v[24:27]\n"
        "v_mfma_f32_16x16x32_bf16 v[28:31], v[80:83], a[164:167], v[28:31]\n"
        "v_mfma_f32_16x16x32_bf16 v[32:35], v[80:83], a[196:199], v[32:35]\n"
        "v_mfma_f32_16x16x32_bf16 v[36:39], v[80:83], a[228:231], v[36:39]\n"
        "s_waitcnt lgkmcnt(1)\n"
        "v_mfma_f32_16x16x32_bf16 v[24:27], v[84:87], a[136:139], v[24:27]\n"
        "v_mfma_f32_16x16x32_bf16 v[28:31], v[84:87], a[168:171], v[28:31]\n"
        "v_mfma_f32_16x16x32_bf16 v[32:35], v[84:87], a[200:203], v[32:35]\n"
        "v_mfma_f32_16x16x32_bf16 v[36:39], v[84:87], a[232:235], v[36:39]\n"
        "s_waitcnt lgkmcnt(0)\n"
        "v_mfma_f32_16x16x32_bf16 v[24:27], v[88:91], a[140:143], v[24:27]\n"
        "v_mfma_f32_16x16x32_bf16 v[28:31], v[88:91], a[172:175], v[28:31]\n"
        "v_mfma_f32_16x16x32_bf16 v[32:35], v[88:91], a[204:207], v[32:35]\n"
        "v_mfma_f32_16x16x32_bf16 v[36:39], v[88:91], a[236:239], v[36:39]\n"
        "s_nop 5\n"
        "v_cvt_pk_bf16_f32 v92, v24, v28\n"
        "v_min_u32 v92, -2, v92\n"
        "v_cvt_pk_bf16_f32 v93, v32, v36\n"
        "v_min_u32 v93, -2, v93\n"
        "buffer_store_dwordx2 v[92:93], v10, s[36:39], s57 offen sc1\n"
        ".Lch_no1_%=:\n"
        "s_bitcmp1_b32 0x3eb, s50\n"
        "s_cbranch_scc0 .Lch_no2_%=\n"
        "v_add_u32 v36, s56, v6\n"
        "v_add_u32 v37, s56, v7\n"
        "ds_read_u16 v38, v36\n"
        "ds_read_u16 v39, v37\n"
        "s_mul_i32 s93, s51, 16384\n"
        "s_add_u32 s93, s93, s62\n"
        "s_add_u32 s94, s93, 4096\n"
        "s_add_u32 s95, s93, 8192\n"
        "s_add_u32 s96, s93, 12288\n"
        "s_waitcnt lgkmcnt(0)\n"
        "v_lshlrev_b32 v38, 16, v38\n"
        "v_lshlrev_b32 v39, 16, v39\n"
        "v_add_u32 v32, s56, v14\n"
        "s_bitcmp1_b32 0x60, s50\n"
        "s_cbranch_scc0 .Lch_y_np_%=\n"
        "s_waitcnt vmcnt(1)\n"
        "v_mfma_f32_16x16x32_bf16 v[24:27], a[248:251], v[224:227], 0\n"
        "v_mfma_f32_16x16x32_bf16 v[24:27], a[248:251], v[228:231], v[24:27]\n"
        "v_mfma_f32_16x16x32_bf16 v[24:27], a[248:251], v[232:235], v[24:27]\n"
        "v_mfma_f32_16x16x32_bf16 v[24:27], a[248:251], v[236:239], v[24:27]\n"
        "s_branch .Lch_y_s_%=\n"
        ".Lch_y_np_%=:\n"
        "s_bitcmp1_b32 0xff80, s50\n"
        "s_cbranch_scc0 .Lch_y_td_%=\n"
        "s_sleep 10\n"
        "s_branch .Lch_y_%=\n"
        ".Lch_y_td_%=:\n"
        "s_waitcnt vmcnt(0)\n"
        "s_bitcmp1_b32 0x7f, s50\n"
        "s_cbranch_scc0 .Lch_y_nd_%=\n"
        "s_sleep 10\n"
        ".Lch_y_nd_%=:\n"
        ".Lch_y_%=:\n"
        "buffer_load_dwordx4 v[224:227], v5, s[36:39], s93 offen sc1\n"
        "buffer_load_dwordx4 v[228:231], v5, s[36:39], s94 offen sc1\n"
        "buffer_load_dwordx4 v[232:235], v5, s[36:39], s95 offen sc1\n"
        "buffer_load_dwordx4 v[236:239], v5, s[36:39], s96 offen sc1\n"
        "s_waitcnt vmcnt(3)\n"
        "v_mfma_f32_16x16x32_bf16 v[24:27], a[248:251], v[224:227], 0\n"
        "s_waitcnt vmcnt(2)\n"
        "v_mfma_f32_16x16x32_bf16 v[24:27], a[248:251], v[228:231], v[24:27]\n"
        "s_waitcnt vmcnt(1)\n"
        "v_mfma_f32_16x16x32_bf16 v[24:27], a[248:251], v[232:235], v[24:27]\n"
        "s_waitcnt vmcnt(0)\n"
        "v_mfma_f32_16x16x32_bf16 v[24:27], a[248:251], v[236:239], v[24:27]\n"
        ".Lch_y_s_%=:\n"
        "s_nop 7\n"
        "v_cmp_u_f32 vcc, v24, v25\n"
        "v_cvt_pk_bf16_f32 v28, v24, v25\n"
        "s_cbranch_vccz .Lch_y_ok_%=\n"
        "v_max3_u32 v29, v224, v225, v226\n"
        "v_max3_u32 v29, v29, v227, v228\n"
        "v_max3_u32 v29, v29, v229, v230\n"
        "v_max3_u32 v29, v29, v231, v232\n"
        "v_max3_u32 v29, v29, v233, v234\n"
        "v_max3_u32 v29, v29, v235, v236\n"
        "v_max3_u32 v29, v29, v237, v238\n"
        "v_max_u32 v29, v29, v239\n"
        "v_cmp_eq_u32 vcc, -1, v29\n"
        "s_cbranch_vccz .Lch_y_ok_%=\n"
        "s_bitcmp1_b32 0x7f, s50\n"
        "s_cbranch_scc0 .Lch_y_ns_%=\n"
        "s_sleep 8\n"
        ".Lch_y_ns_%=:\n"
        "s_add_u32 s55, s55, 1\n"
        "s_cmp_lt_u32 s55, 200000\n"
        "s_cbranch_scc1 .Lch_y_%=\n"
        ".Lch_y_ok_%=:\n"
        "v_lshlrev_b32 v24, 16, v28\n"
        "v_and_b32 v25, 0xffff0000, v28\n"
        "v_pk_add_f32 v[24:25], v[24:25], v[38:39]\n"
        "v_cvt_pk_bf16_f32 v24, v24, v25\n"
        "ds_write_b16 v36, v24\n"
        "ds_write_b16_d16_hi v37, v24\n"
        "s_waitcnt lgkmcnt(0)\n"
        "s_barrier\n"
        "ds_read_b128 v[40:43], v22\n"
        "ds_read_b128 v[32:35], v32\n"
        "ds_read_b128 v[44:47], v22 offset:64\n"
        "ds_read_b128 v[48:51], v22 offset:128\n"
        "ds_read_b128 v[52:55], v22 offset:192\n"
        "ds_read_b128 v[56:59], v22 offset:256\n"
        "ds_read_b128 v[60:63], v22 offset:320\n"
        "ds_read_b128 v[64:67], v22 offset:384\n"
        "ds_read_b128 v[68:71], v22 offset:448\n"
        "ds_read_b128 v[72:75], v22 offset:512\n"
        "ds_read_b128 v[76:79], v22 offset:576\n"
        "ds_read_b128 v[80:83], v22 offset:640\n"
        "ds_read_b128 v[84:87], v22 offset:704\n"
        "ds_read_b128 v[88:91], v22 offset:768\n"
        "ds_read_b128 v[92:95], v22 offset:832\n"
        "s_waitcnt lgkmcnt(14)\n"
        "v_mfma_f32_16x16x32_bf16 v[24:27], v[40:43], v[96:99], 0\n"
        "v_mfma_f32_16x16x32_bf16 v[28:31], v[40:43], v[100:103], 0\n"
        "s_waitcnt lgkmcnt(12)\n"
        "v_mfma_f32_16x16x32_bf16 v[24:27], v[44:47], v[104:107], v[24:27]\n"
        "v_dot2_f32_bf16 v36, v32, s81, 0\n"
        "v_dot2_f32_bf16 v37, v32, v32, 0\n"
        "v_mfma_f32_16x16x32_bf16 v[28:31], v[44:47], v[108:111], v[28:31]\n"
        "v_dot2_f32_bf16 v36, v33, s81, v36\n"
        "v_dot2_f32_bf16 v37, v33, v33, v37\n"
        "s_waitcnt lgkmcnt(11)\n"
        "v_mfma_f32_16x16x32_bf16 v[24:27], v[48:51], v[112:115], v[24:27]\n"
        "v_dot2_f32_bf16 v36, v34, s81, v36\n"
        "v_dot2_f32_bf16 v37, v34, v34, v37\n"
        "v_mfma_f32_16x16x32_bf16 v[28:31], v[48:51], v[116:119], v[28:31]\n"
        "v_dot2_f32_bf16 v36, v35, s81, v36\n"
        "v_dot2_f32_bf16 v37, v35, v35, v37\n"
        "s_waitcnt lgkmcnt(10)\n"
        "v_mfma_f32_16x16x32_bf16 v[24:27], v[52:55], v[120:123], v[24:27]\n"
        "v_mul_f32 v36, v15, v36\n"
        "v_mul_f32 v37, v15, v37\n"
        "v_mfma_f32_16x16x32_bf16 v[28:31], v[52:55], v[124:127], v[28:31]\n"
        "v_add_f32_dpp v36, v36, v36 row_ror:1 row_mask:0xf bank_mask:0xf\n"
        "v_add_f32_dpp v37, v37, v37 row_ror:1 row_mask:0xf bank_mask:0xf\n"
        "s_waitcnt lgkmcnt(9)\n"
        "v_mfma_f32_16x16x32_bf16 v[24:27], v[56:59], v[128:131], v[24:27]\n"
        "v_add_f32_dpp v36, v36, v36 row_ror:2 row_mask:0xf bank_mask:0xf\n"
        "v_add_f32_dpp v37, v37, v37 row_ror:2 row_mask:0xf bank_mask:0xf\n"
        "v_mfma_f32_16x16x32_bf16 v[28:31], v[56:59], v[132:135], v[28:31]\n"
        "v_add_f32_dpp v36, v36, v36 row_ror:4 row_mask:0xf bank_mask:0xf\n"
        "v_add_f32_dpp v37, v37, v37 row_ror:4 row_mask:0xf bank_mask:0xf\n"
        "s_waitcnt lgkmcnt(8)\n"
        "v_mfma_f32_16x16x32_bf16 v[24:27], v[60:63], v[136:139], v[24:27]\n"
        "v_add_f32_dpp v36, v36, v36 row_ror:8 row_mask:0xf bank_mask:0xf\n"
        "v_add_f32_dpp v37, v37, v37 row_ror:8 row_mask:0xf bank_mask:0xf\n"
        "v_mfma_f32_16x16x32_bf16 v[28:31], v[60:63], v[140:143], v[28:31]\n"
        "v_add_f32_dpp v36, v36, v36 row_bcast:15 row_mask:0xa bank_mask:0xf\n"
        "v_add_f32_dpp v37, v37, v37 row_bcast:15 row_mask:0xa bank_mask:0xf\n"
        "s_waitcnt lgkmcnt(7)\n"
        "v_mfma_f32_16x16x32_bf16 v[24:27], v[64:67], v[144:147], v[24:27]\n"
        "v_add_f32_dpp v36, v36, v36 row_bcast:31 row_mask:0xc bank_mask:0xf\n"
        "v_add_f32_dpp v37, v37, v37 row_bcast:31 row_mask:0xc bank_mask:0xf\n"
        "v_mfma_f32_16x16x32_bf16 v[28:31], v[64:67], v[148:151], v[28:31]\n"
        "v_fma_f32 v37, -v36, v36, v37\n"
        "v_max_f32 v37, 0, v37\n"
        "s_waitcnt lgkmcnt(6)\n"
        "v_mfma_f32_16x16x32_bf16 v[24:27], v[68:71], v[152:155], v[24:27]\n"
        "v_add_f32 v37, 0x3727c5ac, v37\n"
        "v_rsq_f32 v37, v37\n"
        "v_mfma_f32_16x16x32_bf16 v[28:31], v[68:71], v[156:159], v[28:31]\n"
        "v_mul_f32 v36, v36, v37\n"
        "v_readlane_b32 s58, v37, 63\n"
        "s_waitcnt lgkmcnt(5)\n"
        "v_mfma_f32_16x16x32_bf16 v[24:27], v[72:75], v[160:163], v[24:27]\n"
        "v_readlane_b32 s59, v36, 63\n"
        "v_mfma_f32_16x16x32_bf16 v[28:31], v[72:75], v[164:167], v[28:31]\n"
        "s_nop 0\n"
        "v_mul_f32 v34, s59, v18\n"
        "s_waitcnt lgkmcnt(0)\n"
        "v_mfma_f32_16x16x32_bf16 v[24:27], v[76:79], v[168:171], v[24:27]\n"
        "v_mul_f32 v35, s59, v19\n"
        "v_mfma_f32_16x16x32_bf16 v[28:31], v[76:79], v[172:175], v[28:31]\n"
        "ds_read_b128 v[40:43], v218 offset:0\n"
        "v_mfma_f32_16x16x32_bf16 v[24:27], v[80:83], v[176:179], v[24:27]\n"
        "ds_read_b128 v[44:47], v218 offset:1024\n"
        "v_mfma_f32_16x16x32_bf16 v[28:31], v[80:83], v[180:183], v[28:31]\n"
        "ds_read_b128 v[48:51], v218 offset:2048\n"
        "v_mfma_f32_16x16x32_bf16 v[24:27], v[84:87], v[184:187], v[24:27]\n"
        "ds_read_b128 v[52:55], v218 offset:3072\n"
        "v_mfma_f32_16x16x32_bf16 v[28:31], v[84:87], v[188:191], v[28:31]\n"
        "ds_read_b128 v[56:59], v218 offset:4096\n"
        "v_mfma_f32_16x16x32_bf16 v[24:27], v[88:91], v[192:195], v[24:27]\n"
        "ds_read_b128 v[60:63], v218 offset:5120\n"
        "v_mfma_f32_16x16x32_bf16 v[28:31], v[88:91], v[196:199], v[28:31]\n"
        "ds_read_b128 v[64:67], v218 offset:6144\n"
        "v_mfma_f32_16x16x32_bf16 v[24:27], v[92:95], v[200:203], v[24:27]\n"
        "v_mfma_f32_16x16x32_bf16 v[28:31], v[92:95], v[204:207], v[28:31]\n"
        "s_nop 6\n"
        "v_fma_f32 v34, v24, s58, -v34\n"
        "v_fma_f32 v35, v28, s58, -v35\n"
        "v_cvt_pk_bf16_f32 v32, v34, v35\n"
        "v_lshlrev_b32 v24, 16, v32\n"
        "v_and_b32 v25, 0xffff0000, v32\n"
        "v_max_f32 v24, 0, v24\n"
        "v_mul_f32 v24, v24, v25\n"
        "v_cvt_pk_bf16_f32 v24, v24, v24\n"
        "ds_write_b16 v13, v24 offset:5120\n"
        "s_mul_i32 s57, s51, 32768\n"
        "s_add_u32 s57, s57, s63\n"
        "s_bitcmp1_b32 0x20, s50\n"
        "s_cbranch_scc0 .Lch_ay1_%=\n"
        "s_and_b32 s52, s68, 7\n"
        "s_mul_i32 s93, s52, 16384\n"
        "s_add_u32 s93, s93, s62\n"
        "s_add_u32 s94, s93, 4096\n"
        "s_add_u32 s95, s93, 8192\n"
        "s_add_u32 s96, s93, 12288\n"
        "buffer_load_dwordx4 v[224:227], v5, s[36:39], s93 offen sc1\n"
        "buffer_load_dwordx4 v[228:231], v5, s[36:39], s94 offen sc1\n"
        "buffer_load_dwordx4 v[232:235], v5, s[36:39], s95 offen sc1\n"
        "buffer_load_dwordx4 v[236:239], v5, s[36:39], s96 offen sc1\n"
        ".Lch_ay1_%=:\n"
        "s_waitcnt lgkmcnt(0)\n"
        "s_barrier\n"
        "ds_read_b128 v[24:27], v4 offset:5120\n"
        "ds_read_b128 v[28:31], v4 offset:5184\n"
        "s_waitcnt lgkmcnt(1)\n"
        "v_mfma_f32_16x16x32_bf16 v[68:71], v[24:27], a[240:243], 0\n"
        "v_mfma_f32_16x16x32_bf16 v[72:75], v[24:27], a[244:247], 0\n"
        "v_mfma_f32_16x16x32_bf16 v[76:79], v[24:27], a[252:255], 0\n"
        "v_mfma_f32_16x16x32_bf16 v[80:83], v[24:27], v[240:243], 0\n"
        "v_mfma_f32_16x16x32_bf16 v[84:87], v[24:27], v[244:247], 0\n"
        "v_mfma_f32_16x16x32_bf16 v[88:91], v[24:27], v[248:251], 0\n"
        "v_mfma_f32_16x16x32_bf16 v[92:95], v[24:27], v[252:255], 0\n"
        "s_waitcnt lgkmcnt(0)\n"
        "v_mfma_f32_16x16x32_bf16 v[68:71], v[28:31], v[40:43], v[68:71]\n"
        "v_mfma_f32_16x16x32_bf16 v[72:75], v[28:31], v[44:47], v[72:75]\n"
        "v_mfma_f32_16x16x32_bf16 v[76:79], v[28:31], v[48:51], v[76:79]\n"
        "v_mfma_f32_16x16x32_bf16 v[80:83], v[28:31], v[52:55], v[80:83]\n"
        "v_mfma_f32_16x16x32_bf16 v[84:87], v[28:31], v[56:59], v[84:87]\n"
        "v_mfma_f32_16x16x32_bf16 v[88:91], v[28:31], v[60:63], v[88:91]\n"
        "v_mfma_f32_16x16x32_bf16 v[92:95], v[28:31], v[64:67], v[92:95]\n"
        "s_nop 2\n"
        "v_cvt_pk_bf16_f32 v32, v68, v72\n"
        "v_min_u32 v32, -2, v32\n"
        "v_cvt_pk_bf16_f32 v33, v76, v80\n"
        "v_min_u32 v33, -2, v33\n"
        "v_cvt_pk_bf16_f32 v34, v84, v88\n"
        "v_min_u32 v34, -2, v34\n"
        "v_cvt_pk_bf16_f32 v35, v92, 0\n"
        "buffer_store_dwordx4 v[32:35], v11, s[36:39], s57 offen sc1\n"
        ".Lch_no2_%=:\n"
        "s_bitcmp1_b32 0x8b, s50\n"
        "s_cbranch_scc0 .Lch_next_%=\n"
        "s_mul_i32 s57, s51, 32768\n"
        "s_add_u32 s57, s57, s65\n"
        "s_add_u32 s57, s57, 131072\n"
        "v_add_u32 v36, s56, v8\n"
        "v_add_u32 v37, s56, v9\n"
        "ds_read_u16 v38, v36\n"
        "ds_read_u16 v39, v37\n"
        "s_mul_i32 s84, s51, 32768\n"
        "s_add_u32 s84, s84, s64\n"
        "s_add_u32 s85, s84, 4096\n"
        "s_add_u32 s86, s84, 8192\n"
        "s_add_u32 s87, s84, 12288\n"
        "s_add_u32 s88, s84, 16384\n"
        "s_add_u32 s89, s84, 20480\n"
        "s_add_u32 s90, s84, 24576\n"
        "s_waitcnt lgkmcnt(0)\n"
        "v_lshlrev_b32 v38, 16, v38\n"
        "v_lshlrev_b32 v39, 16, v39\n"
        "v_add_u32 v32, s56, v14\n"
        "s_bitcmp1_b32 0xff80, s50\n"
        "s_cbranch_scc0 .Lch_z_td_%=\n"
        "s_sleep 10\n"
        "s_branch .Lch_z_%=\n"
        ".Lch_z_td_%=:\n"
        "s_waitcnt vmcnt(0)\n"
        "s_bitcmp1_b32 0x7f, s50\n"
        "s_cbranch_scc0 .Lch_z_nd_%=\n"
        "s_sleep 10\n"
        ".Lch_z_nd_%=:\n"
        ".Lch_z_%=:\n"
        "buffer_load_dwordx4 v[40:43], v5, s[36:39], s84 offen sc1\n"
        "buffer_load_dwordx4 v[44:47], v5, s[36:39], s85 offen sc1\n"
        "buffer_load_dwordx4 v[48:51], v5, s[36:39], s86 offen sc1\n"
        "buffer_load_dwordx4 v[52:55], v5, s[36:39], s87 offen sc1\n"
        "buffer_load_dwordx4 v[56:59], v5, s[36:39], s88 offen sc1\n"
        "buffer_load_dwordx4 v[60:63], v5, s[36:39], s89 offen sc1\n"
        "buffer_load_dwordx4 v[64:67], v5, s[36:39], s90 offen sc1\n"
        "s_waitcnt vmcnt(6)\n"
        "v_mfma_f32_16x16x32_bf16 v[24:27], a[248:251], v[40:43], 0\n"
        "s_waitcnt vmcnt(5)\n"
        "v_mfma_f32_16x16x32_bf16 v[24:27], a[248:251], v[44:47], v[24:27]\n"
        "s_waitcnt vmcnt(4)\n"
        "v_mfma_f32_16x16x32_bf16 v[24:27], a[248:251], v[48:51], v[24:27]\n"
        "s_waitcnt vmcnt(3)\n"
        "v_mfma_f32_16x16x32_bf16 v[24:27], a[248:251], v[52:55], v[24:27]\n"
        "s_waitcnt vmcnt(2)\n"
        "v_mfma_f32_16x16x32_bf16 v[24:27], a[248:251], v[56:59], v[24:27]\n"
        "s_waitcnt vmcnt(1)\n"
        "v_mfma_f32_16x16x32_bf16 v[24:27], a[248:251], v[60:63], v[24:27]\n"
        "s_waitcnt vmcnt(0)\n"
        "v_mfma_f32_16x16x32_bf16 v[24:27], a[248:251], v[64:67], v[24:27]\n"
        ".Lch_z_s_%=:\n"
        "s_nop 7\n"
        "v_cmp_u_f32 vcc, v24, v25\n"
        "v_cvt_pk_bf16_f32 v28, v24, v25\n"
        "s_cbranch_vccz .Lch_z_ok_%=\n"
        "v_max3_u32 v29, v40, v41, v42\n"
        "v_max3_u32 v29, v29, v43, v44\n"
        "v_max3_u32 v29, v29, v45, v46\n"
        "v_max3_u32 v29, v29, v47, v48\n"
        "v_max3_u32 v29, v29, v49, v50\n"
        "v_max3_u32 v29, v29, v51, v52\n"
        "v_max3_u32 v29, v29, v53, v54\n"
        "v_max3_u32 v29, v29, v55, v56\n"
        "v_max3_u32 v29, v29, v57, v58\n"
        "v_max3_u32 v29, v29, v59, v60\n"
        "v_max3_u32 v29, v29, v61, v62\n"
        "v_max3_u32 v29, v29, v63, v64\n"
        "v_max3_u32 v29, v29, v65, v66\n"
        "v_max_u32 v29, v29, v67\n"
        "v_cmp_eq_u32 vcc, -1, v29\n"
        "s_cbranch_vccz .Lch_z_ok_%=\n"
        "s_bitcmp1_b32 0x7f, s50\n"
        "s_cbranch_scc0 .Lch_z_ns_%=\n"
        "s_sleep 8\n"
        ".Lch_z_ns_%=:\n"
        "s_add_u32 s55, s55, 1\n"
        "s_cmp_lt_u32 s55, 200000\n"
        "s_cbranch_scc1 .Lch_z_%=\n"
        ".Lch_z_ok_%=:\n"
        "v_lshlrev_b32 v24, 16, v28\n"
        "v_and_b32 v25, 0xffff0000, v28\n"
        "v_pk_add_f32 v[24:25], v[24:25], v[38:39]\n"
        "v_cvt_pk_bf16_f32 v24, v24, v25\n"
        "ds_write_b16 v36, v24\n"
        "ds_write_b16_d16_hi v37, v24\n"
        "s_waitcnt lgkmcnt(0)\n"
        "s_barrier\n"
        "ds_read_b128 v[40:43], v22\n"
        "ds_read_b128 v[32:35], v32\n"
        "ds_read_b128 v[44:47], v22 offset:64\n"
        "ds_read_b128 v[48:51], v22 offset:128\n"
        "ds_read_b128 v[52:55], v22 offset:192\n"
        "ds_read_b128 v[56:59], v22 offset:256\n"
        "ds_read_b128 v[60:63], v22 offset:320\n"
        "ds_read_b128 v[64:67], v22 offset:384\n"
        "ds_read_b128 v[68:71], v22 offset:448\n"
        "ds_read_b128 v[72:75], v22 offset:512\n"
        "ds_read_b128 v[76:79], v22 offset:576\n"
        "ds_read_b128 v[80:83], v22 offset:640\n"
        "ds_read_b128 v[84:87], v22 offset:704\n"
        "ds_read_b128 v[88:91], v22 offset:768\n"
        "ds_read_b128 v[92:95], v22 offset:832\n"
        "s_bitcmp1_b32 0x8a, s50\n"
        "s_cbranch_scc0 .Lch_ap_%=\n"
        "s_and_b32 s52, s68, 7\n"
        "s_mul_i32 s91, s52, 143360\n"
        "s_add_u32 s91, s91, s60\n"
        "s_add_u32 s92, s91, 5120\n"
        "buffer_load_dwordx2 v[216:217], v0, s[36:39], s91 offen sc1\n"
        "buffer_load_dwordx4 v[208:211], v1, s[36:39], s91 offen sc1\n"
        "buffer_load_dwordx4 v[212:215], v2, s[36:39], s92 offen sc1\n"
        ".Lch_ap_%=:\n"
        "s_bitcmp1_b32 0x8, s50\n"
        "s_cbranch_scc0 .Lch_ay2_%=\n"
        "s_bfe_u32 s52, s68, 0x30004\n"
        "s_mul_i32 s93, s52, 16384\n"
        "s_add_u32 s93, s93, s62\n"
        "s_add_u32 s94, s93, 4096\n"
        "s_add_u32 s95, s93, 8192\n"
        "s_add_u32 s96, s93, 12288\n"
        "buffer_load_dwordx4 v[224:227], v5, s[36:39], s93 offen sc1\n"
        "buffer_load_dwordx4 v[228:231], v5, s[36:39], s94 offen sc1\n"
        "buffer_load_dwordx4 v[232:235], v5, s[36:39], s95 offen sc1\n"
        "buffer_load_dwordx4 v[236:239], v5, s[36:39], s96 offen sc1\n"
        ".Lch_ay2_%=:\n"
        "s_waitcnt lgkmcnt(14)\n"
        "v_mfma_f32_16x16x32_bf16 v[24:27], v[40:43], a[0:3], 0\n"
        "v_mfma_f32_16x16x32_bf16 v[28:31], v[40:43], a[56:59], 0\n"
        "s_waitcnt lgkmcnt(12)\n"
        "v_mfma_f32_16x16x32_bf16 v[24:27], v[44:47], a[4:7], v[24:27]\n"
        "v_dot2_f32_bf16 v36, v32, s81, 0\n"
        "v_dot2_f32_bf16 v37, v32, v32, 0\n"
        "v_mfma_f32_16x16x32_bf16 v[28:31], v[44:47], a[60:63], v[28:31]\n"
        "v_dot2_f32_bf16 v36, v33, s81, v36\n"
        "v_dot2_f32_bf16 v37, v33, v33, v37\n"
        "s_waitcnt lgkmcnt(11)\n"
        "v_mfma_f32_16x16x32_bf16 v[24:27], v[48:51], a[8:11], v[24:27]\n"
        "v_dot2_f32_bf16 v36, v34, s81, v36\n"
        "v_dot2_f32_bf16 v37, v34, v34, v37\n"
        "v_mfma_f32_16x16x32_bf16 v[28:31], v[48:51], a[64:67], v[28:31]\n"
        "v_dot2_f32_bf16 v36, v35, s81, v36\n"
        "v_dot2_f32_bf16 v37, v35, v35, v37\n"
        "s_waitcnt lgkmcnt(10)\n"
        "v_mfma_f32_16x16x32_bf16 v[24:27], v[52:55], a[12:15], v[24:27]\n"
        "v_mul_f32 v36, v15, v36\n"
        "v_mul_f32 v37, v15, v37\n"
        "v_mfma_f32_16x16x32_bf16 v[28:31], v[52:55], a[68:71], v[28:31]\n"
        "v_add_f32_dpp v36, v36, v36 row_ror:1 row_mask:0xf bank_mask:0xf\n"
        "v_add_f32_dpp v37, v37, v37 row_ror:1 row_mask:0xf bank_mask:0xf\n"
        "s_waitcnt lgkmcnt(9)\n"
        "v_mfma_f32_16x16x32_bf16 v[24:27], v[56:59], a[16:19], v[24:27]\n"
        "v_add_f32_dpp v36, v36, v36 row_ror:2 row_mask:0xf bank_mask:0xf\n"
        "v_add_f32_dpp v37, v37, v37 row_ror:2 row_mask:0xf bank_mask:0xf\n"
        "v_mfma_f32_16x16x32_bf16 v[28:31], v[56:59], a[72:75], v[28:31]\n"
        "v_add_f32_dpp v36, v36, v36 row_ror:4 row_mask:0xf bank_mask:0xf\n"
        "v_add_f32_dpp v37, v37, v37 row_ror:4 row_mask:0xf bank_mask:0xf\n"
        "s_waitcnt lgkmcnt(8)\n"
        "v_mfma_f32_16x16x32_bf16 v[24:27], v[60:63], a[20:23], v[24:27]\n"
        "v_add_f32_dpp v36, v36, v36 row_ror:8 row_mask:0xf bank_mask:0xf\n"
        "v_add_f32_dpp v37, v37, v37 row_ror:8 row_mask:0xf bank_mask:0xf\n"
        "v_mfma_f32_16x16x32_bf16 v[28:31], v[60:63], a[76:79], v[28:31]\n"
        "v_add_f32_dpp v36, v36, v36 row_bcast:15 row_mask:0xa bank_mask:0xf\n"
        "v_add_f32_dpp v37, v37, v37 row_bcast:15 row_mask:0xa bank_mask:0xf\n"
        "s_waitcnt lgkmcnt(7)\n"
        "v_mfma_f32_16x16x32_bf16 v[24:27], v[64:67], a[24:27], v[24:27]\n"
        "v_add_f32_dpp v36, v36, v36 row_bcast:31 row_mask:0xc bank_mask:0xf\n"
        "v_add_f32_dpp v37, v37, v37 row_bcast:31 row_mask:0xc bank_mask:0xf\n"
        "v_mfma_f32_16x16x32_bf16 v[28:31], v[64:67], a[80:83], v[28:31]\n"
        "v_fma_f32 v37, -v36, v36, v37\n"
        "v_max_f32 v37, 0, v37\n"
        "s_waitcnt lgkmcnt(6)\n"
        "v_mfma_f32_16x16x32_bf16 v[24:27], v[68:71], a[28:31], v[24:27]\n"
        "v_add_f32 v37, 0x3727c5ac, v37\n"
        "v_rsq_f32 v37, v37\n"
        "v_mfma_f32_16x16x32_bf16 v[28:31], v[68:71], a[84:87], v[28:31]\n"
        "v_mul_f32 v36, v36, v37\n"
        "v_readlane_b32 s58, v37, 63\n"
        "s_waitcnt lgkmcnt(5)\n"
        "v_mfma_f32_16x16x32_bf16 v[24:27], v[72:75], a[32:35], v[24:27]\n"
        "v_readlane_b32 s59, v36, 63\n"
        "v_mfma_f32_16x16x32_bf16 v[28:31], v[72:75], a[88:91], v[28:31]\n"
        "s_nop 0\n"
        "v_mul_f32 v34, s59, v16\n"
        "s_waitcnt lgkmcnt(4)\n"
        "v_mfma_f32_16x16x32_bf16 v[24:27], v[76:79], a[36:39], v[24:27]\n"
        "v_mul_f32 v35, s59, v17\n"
        "v_mfma_f32_16x16x32_bf16 v[28:31], v[76:79], a[92:95], v[28:31]\n"
        "s_waitcnt lgkmcnt(3)\n"
        "v_mfma_f32_16x16x32_bf16 v[24:27], v[80:83], a[40:43], v[24:27]\n"
        "v_mfma_f32_16x16x32_bf16 v[28:31], v[80:83], a[96:99], v[28:31]\n"
        "s_waitcnt lgkmcnt(2)\n"
        "v_mfma_f32_16x16x32_bf16 v[24:27], v[84:87], a[44:47], v[24:27]\n"
        "v_mfma_f32_16x16x32_bf16 v[28:31], v[84:87], a[100:103], v[28:31]\n"
        "s_waitcnt lgkmcnt(1)\n"
        "v_mfma_f32_16x16x32_bf16 v[24:27], v[88:91], a[48:51], v[24:27]\n"
        "v_mfma_f32_16x16x32_bf16 v[28:31], v[88:91], a[104:107], v[28:31]\n"
        "s_waitcnt lgkmcnt(0)\n"
        "v_mfma_f32_16x16x32_bf16 v[24:27], v[92:95], a[52:55], v[24:27]\n"
        "v_mfma_f32_16x16x32_bf16 v[28:31], v[92:95], a[108:111], v[28:31]\n"
        "s_nop 6\n"
        "v_fma_f32 v34, v24, s58, -v34\n"
        "v_fma_f32 v35, v28, s58, -v35\n"
        "v_cvt_pk_bf16_f32 v32, v34, v35\n"
        "s_nop 1\n"
        "v_mov_b32_dpp v33, v32 quad_perm:[1,0,3,2] row_mask:0xf bank_mask:0xf\n"
        "v_perm_b32 v32, v33, v32, v20\n"
        "v_min_u32 v32, -2, v32\n"
        "buffer_store_dword v32, v12, s[36:39], s57 offen sc1\n"
        "s_add_u32 s97, s97, 915\n"
        ".Lch_next_%=:\n"
        "s_add_u32 s50, s50, 1\n"
        "s_cmp_lt_u32 s50, 10\n"
        "s_cbranch_scc1 .Lch_op_%=\n"
        "v_mbcnt_lo_u32_b32 v88, -1, 0\n"
        "v_mbcnt_hi_u32_b32 v88, -1, v88\n"
        "s_lshl_b32 s52, s48, 5\n"
        "s_mul_i32 s84, s49, 32768\n"
        "s_add_u32 s84, s84, s52\n"
        "s_add_u32 s84, s84, 1736704\n"
        "v_bfe_u32 v90, v88, 1, 3\n"
        "v_cmp_gt_u32 vcc, 7, v90\n"
        "v_lshlrev_b32 v90, 12, v90\n"
        "v_lshl_add_u32 v90, v4, 6, v90\n"
        "v_and_b32 v89, 1, v88\n"
        "v_lshl_add_u32 v90, v89, 4, v90\n"
        "v_mov_b32 v91, 1073741824\n"
        "v_cndmask_b32 v90, v91, v90, vcc\n"
        "v_lshl_add_u32 v92, v4, 1, v89\n"
        "s_lshr_b32 s52, s48, 3\n"
        "s_mul_i32 s52, s52, 112\n"
        "s_and_b32 s54, s48, 7\n"
        "s_lshl_b32 s54, s54, 1\n"
        "s_add_u32 s52, s52, s54\n"
        "s_mul_i32 s54, s49, 448\n"
        "s_add_u32 s52, s52, s54\n"
        "v_add_lshl_u32 v92, s52, v92, 1\n"
        "ds_read_u16 v38, v92\n"
        "ds_read_u16 v39, v92 offset:32\n"
        "s_mov_b32 s98, 0x00030003\n"
        "s_mov_b32 s99, 3\n"
        "s_mov_b64 s[40:41], %6\n"
        "s_and_b32 s41, s41, 0xffff\n"
        "s_mov_b32 s42, 3584\n"
        "v_cndmask_b32 v93, v91, v92, s[98:99]\n"
        "s_mov_b32 s99, 0x00030003\n"
        "v_cndmask_b32 v92, v91, v92, s[98:99]\n"
        "s_waitcnt lgkmcnt(0)\n"
        "v_lshlrev_b32 v38, 16, v38\n"
        "v_lshlrev_b32 v39, 16, v39\n"
        "s_sleep 4\n"
        ".Lch_o_%=:\n"
        "buffer_load_dwordx4 v[40:43], v90, s[36:39], s84 offen sc1\n"
        "s_waitcnt vmcnt(0)\n"
        "v_mfma_f32_16x16x32_bf16 v[24:27], a[248:251], v[40:43], 0\n"
        "v_max3_u32 v29, v40, v41, v42\n"
        "v_max_u32 v29, v29, v43\n"
        "v_cmp_eq_u32 vcc, -1, v29\n"
        "s_cbranch_vccz .Lch_o_ok_%=\n"
        "s_add_u32 s55, s55, 1\n"
        "s_cmp_lt_u32 s55, 200000\n"
        "s_cbranch_scc1 .Lch_o_%=\n"
        ".Lch_o_ok_%=:\n"
        "s_nop 3\n"
        "v_add_f32_dpp v24, v24, v24 row_ror:2 row_mask:0xf bank_mask:0xf\n"
        "v_add_f32_dpp v25, v25, v25 row_ror:2 row_mask:0xf bank_mask:0xf\n"
        "s_nop 0\n"
        "v_add_f32_dpp v24, v24, v24 row_ror:4 row_mask:0xf bank_mask:0xf\n"
        "v_add_f32_dpp v25, v25, v25 row_ror:4 row_mask:0xf bank_mask:0xf\n"
        "s_nop 0\n"
        "v_add_f32_dpp v24, v24, v24 row_ror:8 row_mask:0xf bank_mask:0xf\n"
        "v_add_f32_dpp v25, v25, v25 row_ror:8 row_mask:0xf bank_mask:0xf\n"
        "v_cvt_pk_bf16_f32 v28, v24, v25\n"
        "v_lshlrev_b32 v24, 16, v28\n"
        "v_and_b32 v25, 0xffff0000, v28\n"
        "v_pk_add_f32 v[24:25], v[24:25], v[38:39]\n"
        "v_cvt_pk_bf16_f32 v24, v24, v25\n"
        "buffer_store_short v24, v92, s[40:43], 0 offen\n"
        "buffer_store_short_d16_hi v24, v93, s[40:43], 0 offen offset:32\n"
        "s_lshl_b32 s52, s48, 2\n"
        "s_add_u32 s52, s52, s49\n"
        "s_cmp_lg_u32 s52, 3\n"
        "s_cbranch_scc1 .Lch_end_%=\n"
        "v_mov_b32 v25, s35\n"
        "s_mov_b64 exec, 1\n"
        "buffer_store_dword v25, off, s[36:39], 0 offset:0 sc1\n"
        ".Lch_end_%=:\n"
        "s_endpgm\n"
        ".Lfused_code_end:\n"
      :
      : "s"(x), "s"(wq), "s"(wo), "s"(wg), "s"(wu), "s"(wd), "s"(out), "s"(ws), "s"(c), "s"(w), "s"(WS_BYTES), "s"(dbg), "s"(lds0)
      : "memory", "vcc", "scc",
          "s35", "s36", "s37", "s38", "s39", "s40", "s41", "s42", "s43", "s44", "s45", "s46", "s47", "s48", "s49", "s50", "s51", "s52", "s53", "s54", "s55", "s56", "s57", "s58",
          "s59", "s60", "s61", "s62", "s63", "s64", "s65", "s66", "s67", "s68", "s69", "s70", "s71", "s72", "s73", "s74", "s75", "s76", "s77", "s78", "s79", "s80", "s81", "s82",
          "s83", "s84", "s85", "s86", "s87", "s88", "s89", "s90", "s91", "s92", "s93", "s94", "s95", "s96", "s97", "s98", "s99", "v255", "a255"
  );
  __builtin_unreachable();
}

extern "C" void kernel_launch(void* const* d_in, const int* in_sizes, int n_in, void* d_out, int out_size, void* d_ws, size_t ws_size,
                              hipStream_t stream) {
  fused_layers<<<256, 256, 0, stream>>>((const u16*)d_in[0], (const u16*)d_in[1], (const u16*)d_in[2], (const u16*)d_in[3],
                                        (const u16*)d_in[4], (const u16*)d_in[6], (const u16*)d_in[7], (const u16*)d_in[8],
                                        (u16*)d_out, (unsigned char*)d_ws);
}
```
